```python
import torch

def _softmax(x, axis=-1):
    return torch.softmax(x, dim=axis)
def _gelu(x):
    return torch.nn.functional.gelu(x, approximate="tanh")
def _silu(x):
    return torch.nn.functional.silu(x)
def _sigmoid(x):
    return torch.sigmoid(x)
def _layernorm(x, axis=-1, eps=1e-5):
    m = x.mean(dim=axis, keepdim=True)
    v = x.var(dim=axis, keepdim=True, unbiased=False)
    return (x - m) / torch.sqrt(v + eps)
def _scatter_add(tgt, idx, src, axis=0):
    return tgt.clone().index_add_(axis, idx, src)
def _segment_sum(src, seg, n):
    out = torch.zeros((n,) + src.shape[1:], dtype=src.dtype)
    return out.index_add_(0, seg, src)
def _pad(x, pw):
    flat = tuple(v for pair in reversed(pw) for v in pair)
    return torch.nn.functional.pad(x, flat)

kernel_name = 'gated_mix__gqa_block_cached__sliding_block_cached__decode'

def setup_inputs():

    _w = (lambda a: (lambda t: t.to(torch.bfloat16) if t.is_floating_point() else t)(torch.as_tensor(a)))
    def _A_setup():
        B, Sq, D = 2, 1, 512
        Hq, Hkv, Sk, Dh = 32, 32, 4096, 128
        assert Hq % Hkv == 0, (Hq, Hkv)
        return {
            "x": _w((lambda *s: torch.randn(*s, dtype=torch.bfloat16) * 0.05)(B, Sq, D)),
            "k_cache": _w((lambda *s: torch.randn(*s, dtype=torch.bfloat16) * 6.4)(B, Hkv, Sk, Dh)),
            "v_cache": _w((lambda *s: torch.randn(*s, dtype=torch.bfloat16) * 0.05)(B, Hkv, Sk, Dh)),
            "wq": _w((lambda *s: torch.randn(*s, dtype=torch.bfloat16) * 6.4)(D, Hq * Dh) * (D ** -0.5)),
            "wo": _w((lambda *s: torch.randn(*s, dtype=torch.bfloat16) * 0.05)(Hq * Dh, D) * ((Hq * Dh) ** -0.5)),
        }
    _A_in = _A_setup()

    def _B_setup():
        B, Sq, D = 2, 1, 512
        Hq, Hkv, Sk, Dh = 32, 32, 4096, 128
        assert Hq % Hkv == 0, (Hq, Hkv)
        return {
            "x": _w((lambda *s: torch.randn(*s, dtype=torch.bfloat16) * 0.05)(B, Sq, D)),
            "k_cache": _w((lambda *s: torch.randn(*s, dtype=torch.bfloat16) * 6.4)(B, Hkv, Sk, Dh)),
            "v_cache": _w((lambda *s: torch.randn(*s, dtype=torch.bfloat16) * 0.05)(B, Hkv, Sk, Dh)),
            "wq": _w((lambda *s: torch.randn(*s, dtype=torch.bfloat16) * 6.4)(D, Hq * Dh) * (D ** -0.5)),
            "wo": _w((lambda *s: torch.randn(*s, dtype=torch.bfloat16) * 0.05)(Hq * Dh, D) * ((Hq * Dh) ** -0.5)),
        }
    _B_in = _B_setup()

    return {
        "A_x": _A_in["x"],
        "A_k_cache": _A_in["k_cache"],
        "A_v_cache": _A_in["v_cache"],
        "A_wq": _A_in["wq"],
        "A_wo": _A_in["wo"],
        "B_x": _B_in["x"],
        "B_k_cache": _B_in["k_cache"],
        "B_v_cache": _B_in["v_cache"],
        "B_wq": _B_in["wq"],
        "B_wo": _B_in["wo"],
        "gate_w": _w((lambda *s: torch.randn(*s, dtype=torch.bfloat16) * 0.05)(512) * 16.0),
    }

def reference(A_x, A_k_cache, A_v_cache, A_wq, A_wo, B_x, B_k_cache, B_v_cache, B_wq, B_wo, gate_w):
    def _A_ref(x, k_cache, v_cache, wq, wo):
        B, Sq, D = x.shape
        Hq, Dh = 32, 128
        Hkv = k_cache.shape[1]
        rep = Hq // Hkv
        q = (lambda x, *d: x.permute(*d))((x @ wq).reshape(B, Sq, Hq, Dh), 0, 2, 1, 3)
        k = (lambda x, n, axis: torch.repeat_interleave(x, n, dim=axis))(k_cache, rep, axis=1)
        v = (lambda x, n, axis: torch.repeat_interleave(x, n, dim=axis))(v_cache, rep, axis=1)
        scores = torch.einsum("bhqd,bhkd->bhqk", q, k) * (Dh ** -0.5)
        p = _softmax(scores, axis=-1)
        o = torch.einsum("bhqk,bhkd->bhqd", p, v)
        return (lambda x, *d: x.permute(*d))(o, 0, 2, 1, 3).reshape(B, Sq, Hq * Dh) @ wo

    def _B_ref(x, k_cache, v_cache, wq, wo):
        B, Sq, D = x.shape
        Hq, Dh = 32, 128
        Hkv, Sk = k_cache.shape[1], k_cache.shape[2]
        W = min(4096, max(256, Sk // 4))
        rep = Hq // Hkv
        q = (lambda x, *d: x.permute(*d))((x @ wq).reshape(B, Sq, Hq, Dh), 0, 2, 1, 3)
        k = (lambda x, n, axis: torch.repeat_interleave(x, n, dim=axis))(k_cache[:, :, Sk - W:, :], rep, axis=1)
        v = (lambda x, n, axis: torch.repeat_interleave(x, n, dim=axis))(v_cache[:, :, Sk - W:, :], rep, axis=1)
        scores = torch.einsum("bhqd,bhkd->bhqk", q, k) * (Dh ** -0.5)
        p = _softmax(scores, axis=-1)
        o = torch.einsum("bhqk,bhkd->bhqd", p, v)
        return (lambda x, *d: x.permute(*d))(o, 0, 2, 1, 3).reshape(B, Sq, Hq * Dh) @ wo

    x = A_x
    g = _sigmoid((x * gate_w).sum(-1, keepdims=True))
    a = _A_ref(x, A_k_cache, A_v_cache, A_wq, A_wo)
    b = _B_ref(x, B_k_cache, B_v_cache, B_wq, B_wo)
    return g * a + (1 - g) * b
```

```cpp
#include <hip/hip_runtime.h>
#include <hip/hip_bf16.h>
#include <cstdint>

typedef unsigned u32x4 __attribute__((ext_vector_type(4)));
typedef unsigned u32x2 __attribute__((ext_vector_type(2)));
typedef float f32x4 __attribute__((ext_vector_type(4)));
typedef __bf16 bf16x2 __attribute__((ext_vector_type(2)));

constexpr int NH = 32, DH = 128, SK = 4096, WIN = 1024, DM = 512;

__device__ __forceinline__ float dot2(unsigned k, unsigned q, float acc) {
  return __builtin_amdgcn_fdot2_f32_bf16(__builtin_bit_cast(bf16x2, k), __builtin_bit_cast(bf16x2, q), acc, false);
}
template <int CTRL> __device__ __forceinline__ float dpp(float x) {
  return __builtin_bit_cast(float, __builtin_amdgcn_mov_dpp(__builtin_bit_cast(int, x), CTRL, 0xf, 0xf, true));
}
constexpr int XOR1 = 0xB1, XOR2 = 0x4E, XOR7 = 0x141, XOR8 = 0x128, BCAST = 0x150;
__device__ __forceinline__ float blo(unsigned v) { return __builtin_bit_cast(float, v << 16); }
__device__ __forceinline__ float bhi(unsigned v) { return __builtin_bit_cast(float, v & 0xffff0000u); }
__device__ __forceinline__ u32x4 ldnt(const void* p) { return __builtin_nontemporal_load(reinterpret_cast<const u32x4*>(p)); }
__device__ __forceinline__ unsigned short f2bf(float f) { return __builtin_bit_cast(unsigned short, (__bf16)f); }

__device__ __forceinline__ float swap16_add(float a, float b) {
  auto s = __builtin_amdgcn_permlane16_swap(__float_as_uint(a), __float_as_uint(b), false, false);
  return __uint_as_float(s[0]) + __uint_as_float(s[1]);
}
__device__ __forceinline__ float swap32_add(float a, float b) {
  auto s = __builtin_amdgcn_permlane32_swap(__float_as_uint(a), __float_as_uint(b), false, false);
  return __uint_as_float(s[0]) + __uint_as_float(s[1]);
}
__device__ __forceinline__ float wave_max(float x) {
  x = fmaxf(x, dpp<XOR1>(x)); x = fmaxf(x, dpp<XOR2>(x)); x = fmaxf(x, dpp<XOR7>(x)); x = fmaxf(x, dpp<XOR8>(x));
  auto s = __builtin_amdgcn_permlane16_swap(__float_as_uint(x), __float_as_uint(x), false, false);
  x = fmaxf(__uint_as_float(s[0]), __uint_as_float(s[1]));
  auto t = __builtin_amdgcn_permlane32_swap(__float_as_uint(x), __float_as_uint(x), false, false);
  return fmaxf(__uint_as_float(t[0]), __uint_as_float(t[1]));
}
__device__ __forceinline__ float wave_sum(float x) {
  x += dpp<XOR1>(x); x += dpp<XOR2>(x); x += dpp<XOR7>(x); x += dpp<XOR8>(x);
  x = swap16_add(x, x); return swap32_add(x, x);
}

struct P {
  const unsigned short *x, *kA, *vA, *wqA, *woA, *kB, *vB, *wqB, *woB, *gw;
  unsigned short* out;
  float* ws;
};
constexpr int WS_Q = 0, WS_PART = 16384, WS_Y = 16384 + 2 * 2 * 32 * 4 * 132;

__device__ __forceinline__ void decode(int g, int& h, int& j) { h = (g & 7) * 4 + ((g >> 3) & 3); j = g >> 5; }

__global__ void __launch_bounds__(1024) k_q(P p) {
  __shared__ float red[16][2][32];
  int h, j; decode(blockIdx.x, h, j);
  const int X = j >> 2, cs = j & 3, t = threadIdx.x, lane = t & 63, w = t >> 6;
  const unsigned short* wq = (X ? p.wqB : p.wqA) + h * DH + cs * 32 + (t & 3) * 8;
  const int r0 = t >> 2;
  u32x4 a0 = ldnt(wq + (size_t)r0 * 4096), a1 = ldnt(wq + (size_t)(r0 + 256) * 4096);
  float x00 = blo(p.x[r0]), x01 = blo(p.x[r0 + 256]), x10 = blo(p.x[512 + r0]), x11 = blo(p.x[512 + r0 + 256]);
  float acc[2][8];
#pragma unroll
  for (int k = 0; k < 4; ++k) {
    acc[0][2 * k] = x00 * blo(a0[k]) + x01 * blo(a1[k]); acc[0][2 * k + 1] = x00 * bhi(a0[k]) + x01 * bhi(a1[k]);
    acc[1][2 * k] = x10 * blo(a0[k]) + x11 * blo(a1[k]); acc[1][2 * k + 1] = x10 * bhi(a0[k]) + x11 * bhi(a1[k]);
  }
#pragma unroll
  for (int b = 0; b < 2; ++b)
#pragma unroll
    for (int k = 0; k < 8; ++k) { float v = acc[b][k];
      for (int o = 4; o < 64; o <<= 1) v += __shfl_xor(v, o);
      if (lane < 4) red[w][b][lane * 8 + k] = v; }
  __syncthreads();
  if (t < 64) { float s = 0; for (int i = 0; i < 16; ++i) s += red[i][t >> 5][t & 31];
    p.ws[WS_Q + ((X * 2 + (t >> 5)) * 32 + h) * 128 + cs * 32 + (t & 31)] = s; }
}

__global__ void __launch_bounds__(1024) k_attn(P p) {
  __shared__ float part[2][16][128];
  __shared__ float ml[2][16][2];
  int h, j; decode(blockIdx.x, h, j);
  const int b = j >> 2, s = j & 3, t = threadIdx.x, lane = t & 63, w = __builtin_amdgcn_readfirstlane(t >> 6), r = lane >> 4, c = lane & 15;
  const size_t bh = (size_t)(b * NH + h) * SK * DH;
  const size_t offA = bh + (size_t)(s * 1024 + w * 64) * DH + lane * 8, offB = bh + (size_t)(SK - WIN + s * 256 + w * 16) * DH + lane * 8;
  u32x4 kA[16], kB[4], vA[16], vB[4];
#pragma unroll
  for (int i = 0; i < 16; ++i) kA[i] = ldnt(p.kA + offA + i * 512);
#pragma unroll
  for (int i = 0; i < 4; ++i) kB[i] = ldnt(p.kB + offB + i * 512);
  unsigned qA[4], qB[4];
  { const float* qa = p.ws + WS_Q + ((0 * 2 + b) * 32 + h) * 128 + c * 8; const float* qb = p.ws + WS_Q + ((1 * 2 + b) * 32 + h) * 128 + c * 8;
#pragma unroll
    for (int k = 0; k < 4; ++k) { qA[k] = f2bf(qa[2 * k]) | ((unsigned)f2bf(qa[2 * k + 1]) << 16); qB[k] = f2bf(qb[2 * k]) | ((unsigned)f2bf(qb[2 * k + 1]) << 16); } }
  constexpr float C2 = 0.088388347648318440f * 1.4426950408889634f;
  float x[16], y[4];
#pragma unroll
  for (int i = 0; i < 16; ++i) { float d = 0.f;
#pragma unroll
    for (int k = 0; k < 4; ++k) d = dot2(kA[i][k], qA[k], d);
    x[i] = d; vA[i] = ldnt(p.vA + offA + i * 512); }
#pragma unroll
  for (int i = 0; i < 4; ++i) { float d = 0.f;
#pragma unroll
    for (int k = 0; k < 4; ++k) d = dot2(kB[i][k], qB[k], d);
    y[i] = d; vB[i] = ldnt(p.vB + offB + i * 512); }
#define TR(ARR, HALF, CTRL, BIT) _Pragma("unroll") for (int q = 0; q < (HALF); ++q) { const float lo_ = ARR[q], hi_ = ARR[q + (HALF)]; \
    const float t1_ = lo_ + dpp<CTRL>(lo_), t2_ = hi_ + dpp<CTRL>(hi_); ARR[q] = (c & (BIT)) ? t2_ : t1_; }
  TR(x, 8, XOR8, 8) TR(x, 4, XOR7, 4) TR(x, 2, XOR2, 2) TR(x, 1, XOR1, 1)
#pragma unroll
  for (int q = 0; q < 4; ++q) { y[q] += dpp<XOR8>(y[q]); y[q] += dpp<XOR7>(y[q]); }
  TR(y, 2, XOR2, 2) TR(y, 1, XOR1, 1)
  const float sA = x[0] * C2, sB = y[0] * C2;
  const float mA = wave_max(sA), mB = wave_max(sB);
  const float pA = __builtin_amdgcn_exp2f(sA - mA), pB = __builtin_amdgcn_exp2f(sB - mB);
  const float lA = wave_sum(pA), lB = wave_sum(pB) * 0.25f;
  float aA[8], aB[8];
#pragma unroll
  for (int k = 0; k < 8; ++k) { aA[k] = 0.f; aB[k] = 0.f; }
#define PV(ACC, V, PP, I) { const float pb_ = dpp<BCAST + (I)>(PP); _Pragma("unroll") for (int k = 0; k < 4; ++k) { ACC[2 * k] = fmaf(pb_, blo(V[I][k]), ACC[2 * k]); ACC[2 * k + 1] = fmaf(pb_, bhi(V[I][k]), ACC[2 * k + 1]); } }
  PV(aA, vA, pA, 0) PV(aA, vA, pA, 1) PV(aA, vA, pA, 2) PV(aA, vA, pA, 3) PV(aA, vA, pA, 4) PV(aA, vA, pA, 5) PV(aA, vA, pA, 6) PV(aA, vA, pA, 7)
  PV(aA, vA, pA, 8) PV(aA, vA, pA, 9) PV(aA, vA, pA, 10) PV(aA, vA, pA, 11) PV(aA, vA, pA, 12) PV(aA, vA, pA, 13) PV(aA, vA, pA, 14) PV(aA, vA, pA, 15)
  PV(aB, vB, pB, 0) PV(aB, vB, pB, 1) PV(aB, vB, pB, 2) PV(aB, vB, pB, 3)
  float uA[4], uB[4];
#pragma unroll
  for (int k = 0; k < 4; ++k) { uA[k] = swap16_add(aA[k], aA[k + 4]); uB[k] = swap16_add(aB[k], aB[k + 4]); }
  float zA[2], zB[2];
#pragma unroll
  for (int k = 0; k < 2; ++k) { zA[k] = swap32_add(uA[k], uA[k + 2]); zB[k] = swap32_add(uB[k], uB[k + 2]); }
  const int d0 = 8 * c + 4 * (r & 1) + 2 * (r >> 1);
  part[0][w][d0] = zA[0]; part[0][w][d0 + 1] = zA[1]; part[1][w][d0] = zB[0]; part[1][w][d0 + 1] = zB[1];
  if (lane == 0) { ml[0][w][0] = mA; ml[0][w][1] = lA; ml[1][w][0] = mB; ml[1][w][1] = lB; }
  __syncthreads();
  if (t < 256) { const int X = t >> 7, d = t & 127;
    float M = -INFINITY;
#pragma unroll
    for (int i = 0; i < 16; ++i) M = fmaxf(M, ml[X][i][0]);
    float L = 0.f, o = 0.f;
#pragma unroll
    for (int i = 0; i < 16; ++i) { const float e = __builtin_amdgcn_exp2f(ml[X][i][0] - M); L = fmaf(e, ml[X][i][1], L); o = fmaf(e, part[X][i][d], o); }
    float* dst = p.ws + WS_PART + (((X * 2 + b) * 32 + h) * 4 + s) * 132;
    dst[d] = o; if (d == 0) { dst[128] = M; dst[129] = L; }
  }
}

__global__ void __launch_bounds__(1024) k_out(P p) {
  __shared__ float o[2][2][128];
  __shared__ float red[16][2][64];
  __shared__ float gl[2];
  int h, j; decode(blockIdx.x, h, j);
  const int t = threadIdx.x, lane = t & 63, w = t >> 6;
  const int row = t >> 3, pc = t & 7;
  u32x4 wa = ldnt(p.woA + (size_t)(h * 128 + row) * 512 + j * 64 + pc * 8), wb = ldnt(p.woB + (size_t)(h * 128 + row) * 512 + j * 64 + pc * 8);
  if (w >= 14) { const int b = w - 14; float s = 0.f;
    for (int i = lane; i < 512; i += 64) s += blo(p.x[b * 512 + i]) * blo(p.gw[i]);
    s = wave_sum(s); if (lane == 0) gl[b] = 1.f / (1.f + __expf(-s)); }
  if (t < 512) { const int X = t >> 8, b = (t >> 7) & 1, d = t & 127;
    const float* src = p.ws + WS_PART + (((X * 2 + b) * 32 + h) * 4) * 132;
    float M = -INFINITY;
    for (int s = 0; s < 4; ++s) M = fmaxf(M, src[s * 132 + 128]);
    float L = 0.f, a = 0.f;
    for (int s = 0; s < 4; ++s) { const float e = __builtin_amdgcn_exp2f(src[s * 132 + 128] - M); L = fmaf(e, src[s * 132 + 129], L); a = fmaf(e, src[s * 132 + d], a); }
    o[X][b][d] = a / L; }
  __syncthreads();
#pragma unroll
  for (int b = 0; b < 2; ++b) { const float g = gl[b]; const float oa = o[0][b][row] * g, ob = o[1][b][row] * (1.f - g);
#pragma unroll
    for (int k = 0; k < 8; ++k) { float v = (k & 1) ? oa * bhi(wa[k >> 1]) + ob * bhi(wb[k >> 1]) : oa * blo(wa[k >> 1]) + ob * blo(wb[k >> 1]);
      for (int of = 8; of < 64; of <<= 1) v += __shfl_xor(v, of);
      if (lane < 8) red[w][b][lane * 8 + k] = v; } }
  __syncthreads();
  if (t < 128) { float s = 0; for (int i = 0; i < 16; ++i) s += red[i][t >> 6][t & 63];
    p.ws[WS_Y + (h * 2 + (t >> 6)) * 512 + j * 64 + (t & 63)] = s; }
}

__global__ void __launch_bounds__(64) k_fin(P p) {
  const int n = blockIdx.x * 64 + threadIdx.x;
  float s = 0.f;
#pragma unroll
  for (int h = 0; h < 32; ++h) s += p.ws[WS_Y + h * 1024 + n];
  p.out[n] = f2bf(s);
}

extern "C" void kernel_launch(void* const* d_in, const int* in_sizes, int n_in, void* d_out, int out_size, void* d_ws, size_t ws_size, hipStream_t stream) {
  P p;
  p.x = (const unsigned short*)d_in[0]; p.kA = (const unsigned short*)d_in[1]; p.vA = (const unsigned short*)d_in[2];
  p.wqA = (const unsigned short*)d_in[3]; p.woA = (const unsigned short*)d_in[4];
  p.kB = (const unsigned short*)d_in[6]; p.vB = (const unsigned short*)d_in[7]; p.wqB = (const unsigned short*)d_in[8]; p.woB = (const unsigned short*)d_in[9];
  p.gw = (const unsigned short*)d_in[10]; p.out = (unsigned short*)d_out; p.ws = (float*)d_ws;
  k_q<<<256, 1024, 0, stream>>>(p);
  k_attn<<<256, 1024, 0, stream>>>(p);
  k_out<<<256, 1024, 0, stream>>>(p);
  k_fin<<<16, 64, 0, stream>>>(p);
}
```

```cpp
#include <hip/hip_runtime.h>
#include <hip/hip_bf16.h>
#include <cstdint>

typedef unsigned u32x4 __attribute__((ext_vector_type(4)));
typedef unsigned u32x2 __attribute__((ext_vector_type(2)));
typedef float f32x4 __attribute__((ext_vector_type(4)));
typedef float f32x2 __attribute__((ext_vector_type(2)));
typedef __bf16 bf16x2 __attribute__((ext_vector_type(2)));
typedef __amdgpu_buffer_rsrc_t rsrc_t;

#define WIN_K 10
#define POLL_AT 16
#define N0 28
#define WIN_V 10
#define W0 6
#define ILV 1
#define GB_AT 66

constexpr int NT = 2, SC1 = 16;
constexpr unsigned SPIN_CAP = 1u << 20;

__device__ __forceinline__ rsrc_t mk(const void* p, unsigned n) { return __builtin_amdgcn_make_buffer_rsrc((void*)p, 0, n, 0x00020000); }
template <int AUX> __device__ __forceinline__ u32x4 ld4(rsrc_t r, unsigned v, unsigned s) { return __builtin_amdgcn_raw_buffer_load_b128(r, v, s, AUX); }
template <int AUX> __device__ __forceinline__ u32x2 ld2(rsrc_t r, unsigned v, unsigned s) { return __builtin_amdgcn_raw_buffer_load_b64(r, v, s, AUX); }
__device__ __forceinline__ void st4(u32x4 x, rsrc_t r, unsigned v, unsigned s) { __builtin_amdgcn_raw_buffer_store_b128(x, r, v, s, SC1); }
__device__ __forceinline__ float dot2(unsigned k, unsigned q, float acc) {
  return __builtin_amdgcn_fdot2_f32_bf16(__builtin_bit_cast(bf16x2, k), __builtin_bit_cast(bf16x2, q), acc, false);
}
template <int CTRL> __device__ __forceinline__ float dpp(float x) {
  return __builtin_bit_cast(float, __builtin_amdgcn_mov_dpp(__builtin_bit_cast(int, x), CTRL, 0xf, 0xf, true));
}
constexpr int XOR1 = 0xB1, XOR2 = 0x4E, XOR7 = 0x141, XOR8 = 0x128, BCAST = 0x150;
__device__ __forceinline__ float blo(unsigned v) { return __builtin_bit_cast(float, v << 16); }
__device__ __forceinline__ float bhi(unsigned v) { return __builtin_bit_cast(float, v & 0xffff0000u); }
__device__ __forceinline__ f32x2 up2(unsigned v) { f32x2 r; r[0] = blo(v); r[1] = bhi(v); return r; }
__device__ __forceinline__ unsigned f2bf(float f) { return __builtin_bit_cast(unsigned short, (__bf16)f); }
__device__ __forceinline__ unsigned pack2(float a, float b) { return f2bf(a) | (f2bf(b) << 16); }
__device__ __forceinline__ float u2f(unsigned v) { return __builtin_bit_cast(float, v); }
__device__ __forceinline__ unsigned f2u(float v) { return __builtin_bit_cast(unsigned, v); }
__device__ __forceinline__ float swap16_add(float a, float b) {
  auto s = __builtin_amdgcn_permlane16_swap(f2u(a), f2u(b), false, false); return u2f(s[0]) + u2f(s[1]);
}
__device__ __forceinline__ float swap32_add(float a, float b) {
  auto s = __builtin_amdgcn_permlane32_swap(f2u(a), f2u(b), false, false); return u2f(s[0]) + u2f(s[1]);
}
__device__ __forceinline__ float wave_max(float x) {
  x = fmaxf(x, dpp<XOR1>(x)); x = fmaxf(x, dpp<XOR2>(x)); x = fmaxf(x, dpp<XOR7>(x)); x = fmaxf(x, dpp<XOR8>(x));
  auto s = __builtin_amdgcn_permlane16_swap(f2u(x), f2u(x), false, false); x = fmaxf(u2f(s[0]), u2f(s[1]));
  auto t = __builtin_amdgcn_permlane32_swap(f2u(x), f2u(x), false, false); return fmaxf(u2f(t[0]), u2f(t[1]));
}
__device__ __forceinline__ float wave_sum(float x) {
  x += dpp<XOR1>(x); x += dpp<XOR2>(x); x += dpp<XOR7>(x); x += dpp<XOR8>(x);
  x = swap16_add(x, x); return swap32_add(x, x);
}
#define FENCE_SCHED() do { asm volatile("" ::: "memory"); __builtin_amdgcn_sched_barrier(0); } while (0)
template <int N> __device__ __forceinline__ void wait_vm() { asm volatile("s_waitcnt vmcnt(%0)" ::"n"(N) : "memory"); __builtin_amdgcn_sched_barrier(0); }
__device__ __forceinline__ void raw_barrier() { asm volatile("s_barrier" ::: "memory"); __builtin_amdgcn_sched_barrier(0); }
__device__ __forceinline__ void lds_barrier() { asm volatile("s_waitcnt lgkmcnt(0)\n\ts_barrier" ::: "memory"); __builtin_amdgcn_sched_barrier(0); }
#define mid_barrier() lds_barrier()

struct P {
  const void *x, *kA, *vA, *wqA, *woA, *kB, *vB, *wqB, *woB, *gw;
  void* out;
  void* ws;
};
constexpr unsigned WS_EPOCH = 0;
constexpr unsigned WS_Q = 4096;
constexpr unsigned WS_ML = WS_Q + 65536;
constexpr unsigned WS_O = WS_ML + 32768;
constexpr unsigned WS_Y = WS_O + 524288;
constexpr unsigned WS_STAMP = WS_Y + 262144;
constexpr unsigned WS_END = WS_STAMP + 256 * 128;

#define TS(i)

__device__ __forceinline__ u32x4 granule(unsigned a, unsigned c, unsigned T0, unsigned T1) { u32x4 g; g[0] = a; g[1] = T0 ^ c; g[2] = c; g[3] = T1 ^ a; return g; }
__device__ __forceinline__ bool valid(u32x4 g, unsigned T0, unsigned T1) { return ((g[1] ^ g[2]) == T0) & ((g[3] ^ g[0]) == T1); }
__device__ __forceinline__ bool all64(bool v) { return __builtin_amdgcn_ballot_w64(v) == ~0ull; }

#define TR(ARR, O, HALF, CTRL, BIT) _Pragma("unroll") for (int q_ = 0; q_ < (HALF); ++q_) { const float lo_ = ARR[(O) + q_], hi_ = ARR[(O) + q_ + (HALF)]; \
    const float t1_ = lo_ + dpp<CTRL>(lo_), t2_ = hi_ + dpp<CTRL>(hi_); ARR[(O) + q_] = (lane & (BIT)) ? t2_ : t1_; }

template <int I> __device__ __forceinline__ void pv(f32x2 (&acc)[4], const u32x4& v, float p) {
  const float pb = dpp<BCAST + I>(p); const f32x2 pp = {pb, pb};
#pragma unroll
  for (int k = 0; k < 4; ++k) acc[k] = __builtin_elementwise_fma(pp, up2(v[k]), acc[k]);
}
template <int I, int N> struct Unroll { template <class F> static __device__ __forceinline__ void run(F&& f) { f(std::integral_constant<int, I>{}); Unroll<I + 1, N>::run(f); } };
template <int N> struct Unroll<N, N> { template <class F> static __device__ __forceinline__ void run(F&&) {} };

__global__ void __launch_bounds__(512, 2) fused_attn(P p) {
  __shared__ __attribute__((aligned(16))) float redq[16 * 64];
  __shared__ __attribute__((aligned(16))) unsigned qs[128];
  __shared__ __attribute__((aligned(16))) float mls[8 * 4];
  __shared__ __attribute__((aligned(16))) float part[2 * 8 * 128];
  __shared__ __attribute__((aligned(16))) float redy[8 * 128];
  const int g = blockIdx.x, h = (g & 7) * 4 + ((g >> 3) & 3), j = g >> 5, b = j >> 2, s = j & 3;
  const int lane = threadIdx.x & 63, w = __builtin_amdgcn_readfirstlane(threadIdx.x >> 6), r = lane >> 4, c = lane & 15;
  TS(0);
  const rsrc_t rws = mk(p.ws, WS_END), rx = mk(p.x, 2048), rgw = mk(p.gw, 1024);
  const rsrc_t rkA = mk(p.kA, 1u << 26), rvA = mk(p.vA, 1u << 26), rkB = mk(p.kB, 1u << 26), rvB = mk(p.vB, 1u << 26);
  const rsrc_t rwq = mk(b ? p.wqB : p.wqA, 1u << 22), rwoA = mk(p.woA, 1u << 22), rwoB = mk(p.woB, 1u << 22);
  const unsigned l16 = lane * 16;
  const u32x2 ep = ld2<SC1>(rws, 0, WS_EPOCH);
  const u32x2 xq0 = ld2<0>(rx, (lane >> 2) * 8, w * 128), xq1 = ld2<0>(rx, (lane >> 2) * 8, 1024 + w * 128);
  u32x4 wq[4];
  { const unsigned vo = (lane >> 2) * 4 * 8192 + (lane & 3) * 16, so = w * 64 * 8192 + h * 256 + s * 64;
#pragma unroll
    for (int e = 0; e < 4; ++e) wq[e] = ld4<NT>(rwq, vo, so + e * 8192); }
  const u32x4 gx0 = ld4<0>(rx, l16, 0), gx1 = ld4<0>(rx, l16, 1024), ggw = ld4<0>(rgw, l16, 0);
  FENCE_SCHED();
  const unsigned bh = (unsigned)(b * 32 + h) << 20;
  const unsigned oA = bh + (s * 1024 + w * 128) * 256, oB = bh + (3072 + s * 256 + w * 32) * 256;
  const unsigned wov = (lane >> 3) * 2048 + (lane & 7) * 16, wos = (h * 128 + w * 16) * 1024 + j * 128;
  u32x4 S[84];
  auto issue = [&](auto I) { constexpr int i = I;
    if constexpr (i < 8) S[i] = ld4<NT>(rkB, l16, oB + i * 1024);
    else if constexpr (i < 40) S[i] = ld4<NT>(rkA, l16, oA + (i - 8) * 1024);
    else if constexpr (i < 48) S[i] = ld4<NT>(rvB, l16, oB + (i - 40) * 1024);
    else if constexpr (i < 80) S[i] = ld4<NT>(rvA, l16, oA + (i - 48) * 1024);
    else if constexpr (i < 82) S[i] = ld4<NT>(rwoB, wov, wos + (i - 80) * 1024);
    else if constexpr (i < 84) S[i] = ld4<NT>(rwoA, wov, wos + (i - 82) * 1024);
    FENCE_SCHED(); };
  Unroll<0, W0>::run([&](auto I) { constexpr int i = I; if constexpr (i % ILV == 0) raw_barrier(); issue(I); });
  {
    f32x2 a0[4], a1[4];
#pragma unroll
    for (int k = 0; k < 4; ++k) { a0[k] = f32x2{0.f, 0.f}; a1[k] = f32x2{0.f, 0.f}; }
#pragma unroll
    for (int e = 0; e < 4; ++e) {
      const float x0 = (e & 1) ? bhi(xq0[e >> 1]) : blo(xq0[e >> 1]), x1 = (e & 1) ? bhi(xq1[e >> 1]) : blo(xq1[e >> 1]);
#pragma unroll
      for (int k = 0; k < 4; ++k) { const f32x2 wv = up2(wq[e][k]); a0[k] = __builtin_elementwise_fma(f32x2{x0, x0}, wv, a0[k]); a1[k] = __builtin_elementwise_fma(f32x2{x1, x1}, wv, a1[k]); }
    }
    float z[8];
#pragma unroll
    for (int k = 0; k < 8; ++k) z[k] = swap32_add(a0[k >> 1][k & 1], a1[k >> 1][k & 1]);
#pragma unroll
    for (int k = 0; k < 4; ++k) z[k] = swap16_add(z[k], z[k + 4]);
    TR(z, 0, 2, XOR8, 8)
    const int o = (lane >> 5) * 32 + (lane & 3) * 8 + ((lane >> 4) & 1) * 4 + ((lane >> 3) & 1) * 2;
    *(f32x2*)&redq[(w * 2 + ((lane >> 2) & 1)) * 64 + o] = f32x2{z[0], z[1]};
  }
  const unsigned e_lo = __builtin_amdgcn_readfirstlane(ep[0]), e_hi = __builtin_amdgcn_readfirstlane(ep[1]);
  const unsigned long long E1 = (((unsigned long long)e_hi << 32) | e_lo) + 1;
  const unsigned T0 = (unsigned)E1 ^ 0x9E3779B9u, T1 = ((unsigned)(E1 >> 32) ^ 0x7F4A7C15u) + (unsigned)E1 * 0x85EBCA6Bu;
  TS(1);
  mid_barrier();
  if (w == 0) {
    f32x4 a = *(const f32x4*)&redq[(r * 4 + 0) * 64 + c * 4];
#pragma unroll
    for (int i = 1; i < 4; ++i) a += *(const f32x4*)&redq[(r * 4 + i) * 64 + c * 4];
#pragma unroll
    for (int k = 0; k < 4; ++k) { a[k] = swap16_add(a[k], a[k]); a[k] = swap32_add(a[k], a[k]); }
    if (lane < 16) st4(granule(pack2(a[0], a[1]), pack2(a[2], a[3]), T0, T1), rws, (c >> 3) * 32768 + (c & 7) * 16, WS_Q + h * 1024 + j * 128);
  }
  FENCE_SCHED();
  float gate0, gate1;
  { float s0 = 0.f, s1 = 0.f;
#pragma unroll
    for (int k = 0; k < 4; ++k) { s0 = dot2(gx0[k], ggw[k], s0); s1 = dot2(gx1[k], ggw[k], s1); }
    s0 = wave_sum(s0); s1 = wave_sum(s1);
    gate0 = 1.f / (1.f + __expf(-s0)); gate1 = 1.f / (1.f + __expf(-s1)); }
  FENCE_SCHED();
  TS(2);
  Unroll<W0, WIN_K>::run(issue);
  u32x4 qg;
  const unsigned qoff = WS_Q + (b * 32 + h) * 1024;
  Unroll<WIN_K, N0>::run([&](auto I) { constexpr int i = I;
    wait_vm<WIN_K - 1>();
    if constexpr (i == POLL_AT) { TS(3); if (w == 0) qg = ld4<SC1>(rws, l16, qoff); FENCE_SCHED(); }
    issue(I); });
  if (w == 0) {
    unsigned spins = 0;
    if (__builtin_expect(!all64(valid(qg, T0, T1)), 0)) {
      FENCE_SCHED();
#pragma nounroll
      do { FENCE_SCHED(); qg = ld4<SC1>(rws, l16, qoff); } while (!all64(valid(qg, T0, T1)) && ++spins < SPIN_CAP);
      ++spins;
    }
    *(u32x2*)&qs[lane * 2] = u32x2{qg[0], qg[2]};
  }
  TS(4);
  mid_barrier();
  const u32x4 qA = *(const u32x4*)&qs[c * 4], qB = *(const u32x4*)&qs[64 + c * 4];
  constexpr float C2 = 0.088388347648318440f * 1.4426950408889634f;
  float x[40];
  Unroll<0, 40>::run([&](auto I) { constexpr int i = I;
    float d = 0.f;
#pragma unroll
    for (int k = 0; k < 4; ++k) d = dot2(S[i][k], i < 8 ? qB[k] : qA[k], d);
    x[i] = d;
    asm volatile("" : "+v"(x[i]));
    FENCE_SCHED();
    if constexpr (i + WIN_V >= N0) issue(std::integral_constant<int, i + WIN_V>{});
    if constexpr (i == 7) {
#pragma unroll
      for (int q = 0; q < 8; ++q) x[q] += dpp<XOR8>(x[q]);
      TR(x, 0, 4, XOR7, 4) TR(x, 0, 2, XOR2, 2) TR(x, 0, 1, XOR1, 1) FENCE_SCHED(); }
    if constexpr (i == 23) { TR(x, 8, 8, XOR8, 8) TR(x, 8, 4, XOR7, 4) TR(x, 8, 2, XOR2, 2) TR(x, 8, 1, XOR1, 1) FENCE_SCHED(); }
    if constexpr (i == 39) { TR(x, 24, 8, XOR8, 8) TR(x, 24, 4, XOR7, 4) TR(x, 24, 2, XOR2, 2) TR(x, 24, 1, XOR1, 1) FENCE_SCHED(); }
  });
  const float sB = x[0] * C2, sA0 = x[8] * C2, sA1 = x[24] * C2;
  const float mA = wave_max(fmaxf(sA0, sA1)), mB = wave_max(sB);
  const float pA0 = __builtin_amdgcn_exp2f(sA0 - mA), pA1 = __builtin_amdgcn_exp2f(sA1 - mA), pB = __builtin_amdgcn_exp2f(sB - mB);
  const float lA = wave_sum(pA0 + pA1), lB = wave_sum(pB) * 0.5f;
  if (lane == 0) *(f32x4*)&mls[w * 4] = f32x4{mA, lA, mB, lB};
  TS(5);
  FENCE_SCHED();
  f32x2 aA[4], aB[4];
#pragma unroll
  for (int k = 0; k < 4; ++k) { aA[k] = f32x2{0.f, 0.f}; aB[k] = f32x2{0.f, 0.f}; }
  float scA, scB;
  u32x4 gA, gB, gM;
  const int d0 = 8 * c + 4 * (r & 1) + 2 * (r >> 1);
  const unsigned gv = (lane & 7) * 1024 + (lane >> 3) * 16, gsA = WS_O + (h * 2) * 8192 + w * 128, gsB = gsA + 8192;
  const unsigned mv = (lane & 7) * 128 + (lane & 8) * 2, ms = WS_ML + h * 1024;
  Unroll<40, 80>::run([&](auto I) { constexpr int i = I;
    if constexpr (i < 48) pv<i - 40>(aB, S[i], pB);
    else if constexpr (i < 64) pv<i - 48>(aA, S[i], pA0);
    else pv<i - 64>(aA, S[i], pA1);
    if constexpr (i < 48) asm volatile("" : "+v"(aB[0]), "+v"(aB[1]), "+v"(aB[2]), "+v"(aB[3]));
    else asm volatile("" : "+v"(aA[0]), "+v"(aA[1]), "+v"(aA[2]), "+v"(aA[3]));
    FENCE_SCHED();
    if constexpr (i + WIN_V < 84) issue(std::integral_constant<int, i + WIN_V>{});
    if constexpr (i == 42) {
      mid_barrier();
      const f32x4 t = *(const f32x4*)&mls[(lane & 7) * 4];
      float MA = t[0], MB = t[2];
      MA = fmaxf(MA, dpp<XOR1>(MA)); MA = fmaxf(MA, dpp<XOR2>(MA)); MA = fmaxf(MA, dpp<XOR7>(MA));
      MB = fmaxf(MB, dpp<XOR1>(MB)); MB = fmaxf(MB, dpp<XOR2>(MB)); MB = fmaxf(MB, dpp<XOR7>(MB));
      scA = __builtin_amdgcn_exp2f(mA - MA); scB = __builtin_amdgcn_exp2f(mB - MB);
      if (w == 2) {
        float LA = __builtin_amdgcn_exp2f(t[0] - MA) * t[1], LB = __builtin_amdgcn_exp2f(t[2] - MB) * t[3];
        LA += dpp<XOR1>(LA); LA += dpp<XOR2>(LA); LA += dpp<XOR7>(LA);
        LB += dpp<XOR1>(LB); LB += dpp<XOR2>(LB); LB += dpp<XOR7>(LB);
        if (lane < 2) st4(granule(f2u(lane ? MB : MA), f2u(lane ? LB : LA), T0, T1), rws, l16, ms + j * 128);
      }
      FENCE_SCHED();
    }
    if constexpr (i == 47) {
      float u[4];
#pragma unroll
      for (int k = 0; k < 4; ++k) u[k] = swap16_add(aB[k >> 1][k & 1], aB[2 + (k >> 1)][k & 1]);
      *(f32x2*)&part[(8 + w) * 128 + d0] = f32x2{swap32_add(u[0], u[2]) * scB, swap32_add(u[1], u[3]) * scB};
      FENCE_SCHED();
    }
    if constexpr (i == 49) {
      mid_barrier();
      if (w == 1) {
        f32x2 a = *(const f32x2*)&part[8 * 128 + lane * 2];
#pragma unroll
        for (int q = 1; q < 8; ++q) a += *(const f32x2*)&part[(8 + q) * 128 + lane * 2];
        st4(granule(f2u(a[0]), f2u(a[1]), T0, T1), rws, l16, WS_O + (h * 2 + 1) * 8192 + j * 1024);
      }
      FENCE_SCHED();
    }
    if constexpr (i == GB_AT) { gB = ld4<SC1>(rws, gv, gsB); gM = ld4<SC1>(rws, mv, ms); FENCE_SCHED(); }
  });
  TS(6);
  { float u[4];
#pragma unroll
    for (int k = 0; k < 4; ++k) u[k] = swap16_add(aA[k >> 1][k & 1], aA[2 + (k >> 1)][k & 1]);
    *(f32x2*)&part[w * 128 + d0] = f32x2{swap32_add(u[0], u[2]) * scA, swap32_add(u[1], u[3]) * scA}; }
  lds_barrier();
  if (w == 0) {
    f32x2 a = *(const f32x2*)&part[lane * 2];
#pragma unroll
    for (int q = 1; q < 8; ++q) a += *(const f32x2*)&part[q * 128 + lane * 2];
    st4(granule(f2u(a[0]), f2u(a[1]), T0, T1), rws, l16, WS_O + (h * 2) * 8192 + j * 1024);
  }
  FENCE_SCHED();
  TS(7);
  unsigned spins = 0;
  if (__builtin_expect(!all64(valid(gB, T0, T1) & valid(gM, T0, T1)), 0)) {
#pragma nounroll
    do { FENCE_SCHED(); __builtin_amdgcn_s_sleep(4); gB = ld4<SC1>(rws, gv, gsB); gM = ld4<SC1>(rws, mv, ms);
    } while (!all64(valid(gB, T0, T1) & valid(gM, T0, T1)) && ++spins < SPIN_CAP);
  }
  f32x2 y0[4], y1[4], wa[8]; float wA;
  {
    const float m = u2f(gM[0]); float M = fmaxf(m, dpp<XOR1>(m)); M = fmaxf(M, dpp<XOR2>(M));
    const float e = __builtin_amdgcn_exp2f(m - M); float L = e * u2f(gM[2]); L += dpp<XOR1>(L); L += dpp<XOR2>(L);
    const float gt = (lane & 4) ? gate1 : gate0;
    const float wt = e / L * ((lane & 8) ? 1.f - gt : gt), wo_ = dpp<XOR8>(wt);
    wA = (lane & 8) ? wo_ : wt; const float wB = (lane & 8) ? wt : wo_;
#pragma unroll
    for (int k = 0; k < 4; ++k) { y0[k] = f32x2{0.f, 0.f}; y1[k] = f32x2{0.f, 0.f}; }
#pragma unroll
    for (int e2 = 0; e2 < 2; ++e2) {
      float v = u2f(gB[2 * e2]) * wB; v += dpp<XOR1>(v); v += dpp<XOR2>(v);
      const float vo = dpp<XOR7>(v);
#pragma unroll
      for (int k = 0; k < 4; ++k) { const f32x2 wv = up2(S[80 + e2][k]); y0[k] = __builtin_elementwise_fma(f32x2{v, v}, wv, y0[k]); y1[k] = __builtin_elementwise_fma(f32x2{vo, vo}, wv, y1[k]); }
    }
#pragma unroll
    for (int k = 0; k < 8; ++k) { wa[k] = up2(S[82 + (k >> 2)][k & 3]); asm volatile("" : "+v"(wa[k])); }
#pragma unroll
    for (int k = 0; k < 4; ++k) asm volatile("" : "+v"(y0[k]), "+v"(y1[k]));
    asm volatile("" : "+v"(wA));
  }
  FENCE_SCHED();
#pragma nounroll
  do { FENCE_SCHED(); gA = ld4<SC1>(rws, gv, gsA); } while (!all64(valid(gA, T0, T1)) && ++spins < SPIN_CAP);
  TS(8);
  {
#pragma unroll
    for (int e2 = 0; e2 < 2; ++e2) {
      float v = u2f(gA[2 * e2]) * wA; v += dpp<XOR1>(v); v += dpp<XOR2>(v);
      const float vo = dpp<XOR7>(v);
#pragma unroll
      for (int k = 0; k < 4; ++k) { y0[k] = __builtin_elementwise_fma(f32x2{v, v}, wa[e2 * 4 + k], y0[k]); y1[k] = __builtin_elementwise_fma(f32x2{vo, vo}, wa[e2 * 4 + k], y1[k]); }
    }
    float z[8];
#pragma unroll
    for (int k = 0; k < 8; ++k) z[k] = swap32_add(y0[k >> 1][k & 1], y1[k >> 1][k & 1]);
#pragma unroll
    for (int k = 0; k < 4; ++k) z[k] = swap16_add(z[k], z[k + 4]);
    TR(z, 0, 2, XOR8, 8)
    const int o2 = (((lane >> 5) ^ (lane >> 2)) & 1) * 64 + (lane & 7) * 8 + ((lane >> 4) & 1) * 4 + ((lane >> 3) & 1) * 2;
    *(f32x2*)&redy[w * 128 + o2] = f32x2{z[0], z[1]};
  }
  lds_barrier();
  if (w == 0) {
    f32x2 a = *(const f32x2*)&redy[lane * 2];
#pragma unroll
    for (int i = 1; i < 8; ++i) a += *(const f32x2*)&redy[i * 128 + lane * 2];
    st4(granule(f2u(a[0]), f2u(a[1]), T0, T1), rws, l16, WS_Y + j * 32768 + h * 1024);
    FENCE_SCHED();
    TS(9);
    u32x4 gy;
#pragma nounroll
    do { FENCE_SCHED(); gy = ld4<SC1>(rws, (lane >> 1) * 1024 + (lane & 1) * 16, WS_Y + j * 32768 + h * 32); } while (!all64(valid(gy, T0, T1)) && ++spins < SPIN_CAP);
    float y0 = u2f(gy[0]), y1 = u2f(gy[2]);
    y0 += dpp<XOR2>(y0); y1 += dpp<XOR2>(y1);
    y0 += dpp<0x124>(y0); y1 += dpp<0x124>(y1);
    y0 += dpp<XOR8>(y0); y1 += dpp<XOR8>(y1);
    y0 = swap16_add(y0, y0); y1 = swap16_add(y1, y1);
    y0 = swap32_add(y0, y0); y1 = swap32_add(y1, y1);
    const int gl = 2 * h + lane;
    if (lane < 2) ((unsigned*)p.out)[(gl >> 5) * 256 + j * 32 + (gl & 31)] = spins < SPIN_CAP ? pack2(y0, y1) : 0x7fc07fc0u;
    if (g == 0 && lane == 0) __builtin_amdgcn_raw_buffer_store_b64(u32x2{(unsigned)E1, (unsigned)(E1 >> 32)}, rws, 0, WS_EPOCH, SC1);
    TS(10);
  }
}

extern "C" void kernel_launch(void* const* d_in, const int* in_sizes, int n_in, void* d_out, int out_size, void* d_ws, size_t ws_size, hipStream_t stream) {
  P p;
  p.x = d_in[0]; p.kA = d_in[1]; p.vA = d_in[2]; p.wqA = d_in[3]; p.woA = d_in[4];
  p.kB = d_in[6]; p.vB = d_in[7]; p.wqB = d_in[8]; p.woB = d_in[9]; p.gw = d_in[10];
  p.out = d_out; p.ws = d_ws;
  fused_attn<<<256, 512, 0, stream>>>(p);
}
```

```cpp
#include <hip/hip_runtime.h>
#include <hip/hip_bf16.h>
#include <cstdint>

typedef unsigned u32x4 __attribute__((ext_vector_type(4)));
typedef unsigned u32x2 __attribute__((ext_vector_type(2)));
typedef float f32x4 __attribute__((ext_vector_type(4)));
typedef float f32x2 __attribute__((ext_vector_type(2)));
typedef __bf16 bf16x2 __attribute__((ext_vector_type(2)));
typedef __amdgpu_buffer_rsrc_t rsrc_t;

#define WIN_K 10
#define POLL_AT 16
#define N0 28
#define WIN_V 10
#define W0 6
#define ILV 1
#define GB_AT 66

constexpr int NT = 2, SC1 = 16;
constexpr unsigned SPIN_CAP = 1u << 20;

__device__ __forceinline__ rsrc_t mk(const void* p, unsigned n) { return __builtin_amdgcn_make_buffer_rsrc((void*)p, 0, n, 0x00020000); }
template <int AUX> __device__ __forceinline__ u32x4 ld4(rsrc_t r, unsigned v, unsigned s) { return __builtin_amdgcn_raw_buffer_load_b128(r, v, s, AUX); }
template <int AUX> __device__ __forceinline__ u32x2 ld2(rsrc_t r, unsigned v, unsigned s) { return __builtin_amdgcn_raw_buffer_load_b64(r, v, s, AUX); }
__device__ __forceinline__ void st4(u32x4 x, rsrc_t r, unsigned v, unsigned s) { __builtin_amdgcn_raw_buffer_store_b128(x, r, v, s, SC1); }
__device__ __forceinline__ float dot2(unsigned k, unsigned q, float acc) {
  return __builtin_amdgcn_fdot2_f32_bf16(__builtin_bit_cast(bf16x2, k), __builtin_bit_cast(bf16x2, q), acc, false);
}
template <int CTRL> __device__ __forceinline__ float dpp(float x) {
  return __builtin_bit_cast(float, __builtin_amdgcn_mov_dpp(__builtin_bit_cast(int, x), CTRL, 0xf, 0xf, true));
}
constexpr int XOR1 = 0xB1, XOR2 = 0x4E, XOR7 = 0x141, XOR8 = 0x128, BCAST = 0x150;
__device__ __forceinline__ float blo(unsigned v) { return __builtin_bit_cast(float, v << 16); }
__device__ __forceinline__ float bhi(unsigned v) { return __builtin_bit_cast(float, v & 0xffff0000u); }
__device__ __forceinline__ f32x2 up2(unsigned v) { f32x2 r; r[0] = blo(v); r[1] = bhi(v); return r; }
__device__ __forceinline__ unsigned f2bf(float f) { return __builtin_bit_cast(unsigned short, (__bf16)f); }
__device__ __forceinline__ unsigned pack2(float a, float b) { return f2bf(a) | (f2bf(b) << 16); }
__device__ __forceinline__ float u2f(unsigned v) { return __builtin_bit_cast(float, v); }
__device__ __forceinline__ unsigned f2u(float v) { return __builtin_bit_cast(unsigned, v); }
__device__ __forceinline__ float swap16_add(float a, float b) {
  auto s = __builtin_amdgcn_permlane16_swap(f2u(a), f2u(b), false, false); return u2f(s[0]) + u2f(s[1]);
}
__device__ __forceinline__ float swap32_add(float a, float b) {
  auto s = __builtin_amdgcn_permlane32_swap(f2u(a), f2u(b), false, false); return u2f(s[0]) + u2f(s[1]);
}
__device__ __forceinline__ float wave_max(float x) {
  x = fmaxf(x, dpp<XOR1>(x)); x = fmaxf(x, dpp<XOR2>(x)); x = fmaxf(x, dpp<XOR7>(x)); x = fmaxf(x, dpp<XOR8>(x));
  auto s = __builtin_amdgcn_permlane16_swap(f2u(x), f2u(x), false, false); x = fmaxf(u2f(s[0]), u2f(s[1]));
  auto t = __builtin_amdgcn_permlane32_swap(f2u(x), f2u(x), false, false); return fmaxf(u2f(t[0]), u2f(t[1]));
}
__device__ __forceinline__ float wave_sum(float x) {
  x += dpp<XOR1>(x); x += dpp<XOR2>(x); x += dpp<XOR7>(x); x += dpp<XOR8>(x);
  x = swap16_add(x, x); return swap32_add(x, x);
}
#define FENCE_SCHED() do { asm volatile("" ::: "memory"); __builtin_amdgcn_sched_barrier(0); } while (0)
template <int N> __device__ __forceinline__ void wait_vm() { asm volatile("s_waitcnt vmcnt(%0)" ::"n"(N) : "memory"); __builtin_amdgcn_sched_barrier(0); }
__device__ __forceinline__ void raw_barrier() { asm volatile("s_barrier" ::: "memory"); __builtin_amdgcn_sched_barrier(0); }
__device__ __forceinline__ void lds_barrier() { asm volatile("s_waitcnt lgkmcnt(0)\n\ts_barrier" ::: "memory"); __builtin_amdgcn_sched_barrier(0); }
#define mid_barrier() lds_barrier()

struct P {
  const void *x, *kA, *vA, *wqA, *woA, *kB, *vB, *wqB, *woB, *gw;
  void* out;
  void* ws;
};
constexpr unsigned WS_EPOCH = 0;
constexpr unsigned WS_Q = 4096;
constexpr unsigned WS_ML = WS_Q + 65536;
constexpr unsigned WS_O = WS_ML + 32768;
constexpr unsigned WS_Y = WS_O + 524288;
constexpr unsigned WS_STAMP = WS_Y + 262144;
constexpr unsigned WS_END = WS_STAMP + 256 * 128;

#define TS(i)

__device__ __forceinline__ u32x4 granule(unsigned a, unsigned c, unsigned T0, unsigned T1) { u32x4 g; g[0] = a; g[1] = T0 ^ c; g[2] = c; g[3] = T1 ^ a; return g; }
__device__ __forceinline__ bool valid(u32x4 g, unsigned T0, unsigned T1) { return ((g[1] ^ g[2]) == T0) & ((g[3] ^ g[0]) == T1); }
__device__ __forceinline__ bool all64(bool v) { return __builtin_amdgcn_ballot_w64(v) == ~0ull; }

#define TR(ARR, O, HALF, CTRL, BIT) _Pragma("unroll") for (int q_ = 0; q_ < (HALF); ++q_) { const float lo_ = ARR[(O) + q_], hi_ = ARR[(O) + q_ + (HALF)]; \
    const float t1_ = lo_ + dpp<CTRL>(lo_), t2_ = hi_ + dpp<CTRL>(hi_); ARR[(O) + q_] = (lane & (BIT)) ? t2_ : t1_; }

template <int I> __device__ __forceinline__ void pv(f32x2 (&acc)[4], const u32x4& v, float p) {
  const float pb = dpp<BCAST + I>(p); const f32x2 pp = {pb, pb};
#pragma unroll
  for (int k = 0; k < 4; ++k) acc[k] = __builtin_elementwise_fma(pp, up2(v[k]), acc[k]);
}
template <int I, int N> struct Unroll { template <class F> static __device__ __forceinline__ void run(F&& f) { f(std::integral_constant<int, I>{}); Unroll<I + 1, N>::run(f); } };
template <int N> struct Unroll<N, N> { template <class F> static __device__ __forceinline__ void run(F&&) {} };

__global__ void __launch_bounds__(512, 2) fused_attn(P p) {
  __shared__ __attribute__((aligned(16))) float redq[16 * 64];
  __shared__ __attribute__((aligned(16))) unsigned qs[128];
  __shared__ __attribute__((aligned(16))) float mls[8 * 4];
  __shared__ __attribute__((aligned(16))) float part[2 * 8 * 128];
  __shared__ __attribute__((aligned(16))) float redy[8 * 128];
  const int g = blockIdx.x, h = (g & 7) * 4 + ((g >> 3) & 3), j = g >> 5, b = j >> 2, s = j & 3;
  const int lane = threadIdx.x & 63, w = __builtin_amdgcn_readfirstlane(threadIdx.x >> 6), r = lane >> 4, c = lane & 15;
  TS(0);
  const rsrc_t rws = mk(p.ws, WS_END), rx = mk(p.x, 2048), rgw = mk(p.gw, 1024);
  const rsrc_t rkA = mk(p.kA, 1u << 26), rvA = mk(p.vA, 1u << 26), rkB = mk(p.kB, 1u << 26), rvB = mk(p.vB, 1u << 26);
  const rsrc_t rwq = mk(b ? p.wqB : p.wqA, 1u << 22), rwoA = mk(p.woA, 1u << 22), rwoB = mk(p.woB, 1u << 22);
  const unsigned l16 = lane * 16;
  const u32x2 ep = ld2<SC1>(rws, 0, WS_EPOCH);
  const u32x2 xq0 = ld2<0>(rx, (lane >> 2) * 8, w * 128), xq1 = ld2<0>(rx, (lane >> 2) * 8, 1024 + w * 128);
  u32x4 wq[4];
  { const unsigned vo = (lane >> 2) * 4 * 8192 + (lane & 3) * 16, so = w * 64 * 8192 + h * 256 + s * 64;
#pragma unroll
    for (int e = 0; e < 4; ++e) wq[e] = ld4<NT>(rwq, vo, so + e * 8192); }
  const u32x4 gx0 = ld4<0>(rx, l16, 0), gx1 = ld4<0>(rx, l16, 1024), ggw = ld4<0>(rgw, l16, 0);
  FENCE_SCHED();
  const unsigned bh = (unsigned)(b * 32 + h) << 20;
  const unsigned oA = bh + (s * 1024 + w * 128) * 256, oB = bh + (3072 + s * 256 + w * 32) * 256;
  const unsigned wov = (lane >> 3) * 2048 + (lane & 7) * 16, wos = (h * 128 + w * 16) * 1024 + j * 128;
  u32x4 S[84];
  auto issue = [&](auto I) { constexpr int i = I;
    if constexpr (i < 8) S[i] = ld4<NT>(rkB, l16, oB + i * 1024);
    else if constexpr (i < 40) S[i] = ld4<NT>(rkA, l16, oA + (i - 8) * 1024);
    else if constexpr (i < 48) S[i] = ld4<NT>(rvB, l16, oB + (i - 40) * 1024);
    else if constexpr (i < 80) S[i] = ld4<NT>(rvA, l16, oA + (i - 48) * 1024);
    else if constexpr (i < 82) S[i] = ld4<NT>(rwoB, wov, wos + (i - 80) * 1024);
    else if constexpr (i < 84) S[i] = ld4<NT>(rwoA, wov, wos + (i - 82) * 1024);
    FENCE_SCHED(); };
  Unroll<0, W0>::run([&](auto I) { constexpr int i = I; if constexpr (i % ILV == 0) raw_barrier(); issue(I); });
  {
    f32x2 a0[4], a1[4];
#pragma unroll
    for (int k = 0; k < 4; ++k) { a0[k] = f32x2{0.f, 0.f}; a1[k] = f32x2{0.f, 0.f}; }
#pragma unroll
    for (int e = 0; e < 4; ++e) {
      const float x0 = (e & 1) ? bhi(xq0[e >> 1]) : blo(xq0[e >> 1]), x1 = (e & 1) ? bhi(xq1[e >> 1]) : blo(xq1[e >> 1]);
#pragma unroll
      for (int k = 0; k < 4; ++k) { const f32x2 wv = up2(wq[e][k]); a0[k] = __builtin_elementwise_fma(f32x2{x0, x0}, wv, a0[k]); a1[k] = __builtin_elementwise_fma(f32x2{x1, x1}, wv, a1[k]); }
    }
    float z[8];
#pragma unroll
    for (int k = 0; k < 8; ++k) z[k] = swap32_add(a0[k >> 1][k & 1], a1[k >> 1][k & 1]);
#pragma unroll
    for (int k = 0; k < 4; ++k) z[k] = swap16_add(z[k], z[k + 4]);
    TR(z, 0, 2, XOR8, 8)
    const int o = (lane >> 5) * 32 + (lane & 3) * 8 + ((lane >> 4) & 1) * 4 + ((lane >> 3) & 1) * 2;
    *(f32x2*)&redq[(w * 2 + ((lane >> 2) & 1)) * 64 + o] = f32x2{z[0], z[1]};
  }
  const unsigned e_lo = __builtin_amdgcn_readfirstlane(ep[0]), e_hi = __builtin_amdgcn_readfirstlane(ep[1]);
  const unsigned long long E1 = (((unsigned long long)e_hi << 32) | e_lo) + 1;
  const unsigned T0 = (unsigned)E1 ^ 0x9E3779B9u, T1 = ((unsigned)(E1 >> 32) ^ 0x7F4A7C15u) + (unsigned)E1 * 0x85EBCA6Bu;
  TS(1);
  mid_barrier();
  if (w == 0) {
    f32x4 a = *(const f32x4*)&redq[(r * 4 + 0) * 64 + c * 4];
#pragma unroll
    for (int i = 1; i < 4; ++i) a += *(const f32x4*)&redq[(r * 4 + i) * 64 + c * 4];
#pragma unroll
    for (int k = 0; k < 4; ++k) { a[k] = swap16_add(a[k], a[k]); a[k] = swap32_add(a[k], a[k]); }
    if (lane < 16) st4(granule(pack2(a[0], a[1]), pack2(a[2], a[3]), T0, T1), rws, (c >> 3) * 32768 + (c & 7) * 16, WS_Q + h * 1024 + j * 128);
  }
  FENCE_SCHED();
  float gate0, gate1;
  { float s0 = 0.f, s1 = 0.f;
#pragma unroll
    for (int k = 0; k < 4; ++k) { s0 = dot2(gx0[k], ggw[k], s0); s1 = dot2(gx1[k], ggw[k], s1); }
    s0 = wave_sum(s0); s1 = wave_sum(s1);
    gate0 = 1.f / (1.f + __expf(-s0)); gate1 = 1.f / (1.f + __expf(-s1)); }
  FENCE_SCHED();
  TS(2);
  Unroll<W0, WIN_K>::run(issue);
  u32x4 qg;
  const unsigned qoff = WS_Q + (b * 32 + h) * 1024;
  Unroll<WIN_K, N0>::run([&](auto I) { constexpr int i = I;
    wait_vm<WIN_K - 1>();
    if constexpr (i == POLL_AT) { TS(3); if (w == 0) qg = ld4<SC1>(rws, l16, qoff); FENCE_SCHED(); }
    issue(I); });
  if (w == 0) {
    unsigned spins = 0;
    if (__builtin_expect(!all64(valid(qg, T0, T1)), 0)) {
      FENCE_SCHED();
#pragma nounroll
      do { FENCE_SCHED(); qg = ld4<SC1>(rws, l16, qoff); } while (!all64(valid(qg, T0, T1)) && ++spins < SPIN_CAP);
      ++spins;
    }
    *(u32x2*)&qs[lane * 2] = u32x2{qg[0], qg[2]};
  }
  TS(4);
  mid_barrier();
  const u32x4 qA = *(const u32x4*)&qs[c * 4], qB = *(const u32x4*)&qs[64 + c * 4];
  constexpr float C2 = 0.088388347648318440f * 1.4426950408889634f;
  float x[40];
  Unroll<0, 40>::run([&](auto I) { constexpr int i = I;
    float d = 0.f;
#pragma unroll
    for (int k = 0; k < 4; ++k) d = dot2(S[i][k], i < 8 ? qB[k] : qA[k], d);
    x[i] = d;
    asm volatile("" : "+v"(x[i]));
    FENCE_SCHED();
    if constexpr (i + WIN_V >= N0) issue(std::integral_constant<int, i + WIN_V>{});
    if constexpr (i == 7) {
#pragma unroll
      for (int q = 0; q < 8; ++q) x[q] += dpp<XOR8>(x[q]);
      TR(x, 0, 4, XOR7, 4) TR(x, 0, 2, XOR2, 2) TR(x, 0, 1, XOR1, 1) FENCE_SCHED(); }
    if constexpr (i == 23) { TR(x, 8, 8, XOR8, 8) TR(x, 8, 4, XOR7, 4) TR(x, 8, 2, XOR2, 2) TR(x, 8, 1, XOR1, 1) FENCE_SCHED(); }
    if constexpr (i == 39) { TR(x, 24, 8, XOR8, 8) TR(x, 24, 4, XOR7, 4) TR(x, 24, 2, XOR2, 2) TR(x, 24, 1, XOR1, 1) FENCE_SCHED(); }
  });
  const float sB = x[0] * C2, sA0 = x[8] * C2, sA1 = x[24] * C2;
  const float mA = wave_max(fmaxf(sA0, sA1)), mB = wave_max(sB);
  const float pA0 = __builtin_amdgcn_exp2f(sA0 - mA), pA1 = __builtin_amdgcn_exp2f(sA1 - mA), pB = __builtin_amdgcn_exp2f(sB - mB);
  const float lA = wave_sum(pA0 + pA1), lB = wave_sum(pB) * 0.5f;
  if (lane == 0) *(f32x4*)&mls[w * 4] = f32x4{mA, lA, mB, lB};
  TS(5);
  FENCE_SCHED();
  f32x2 aA[4], aB[4];
#pragma unroll
  for (int k = 0; k < 4; ++k) { aA[k] = f32x2{0.f, 0.f}; aB[k] = f32x2{0.f, 0.f}; }
  float scA, scB;
  u32x4 gA, gB, gM;
  const int d0 = 8 * c + 4 * (r & 1) + 2 * (r >> 1);
  const unsigned gv = (lane & 7) * 1024 + (lane >> 3) * 16, gsA = WS_O + (h * 2) * 8192 + w * 128, gsB = gsA + 8192;
  const unsigned mv = (lane & 7) * 128 + (lane & 8) * 2, ms = WS_ML + h * 1024;
  Unroll<40, 80>::run([&](auto I) { constexpr int i = I;
    if constexpr (i < 48) pv<i - 40>(aB, S[i], pB);
    else if constexpr (i < 64) pv<i - 48>(aA, S[i], pA0);
    else pv<i - 64>(aA, S[i], pA1);
    if constexpr (i < 48) asm volatile("" : "+v"(aB[0]), "+v"(aB[1]), "+v"(aB[2]), "+v"(aB[3]));
    else asm volatile("" : "+v"(aA[0]), "+v"(aA[1]), "+v"(aA[2]), "+v"(aA[3]));
    FENCE_SCHED();
    if constexpr (i + WIN_V < 84) issue(std::integral_constant<int, i + WIN_V>{});
    if constexpr (i == 42) {
      mid_barrier();
      const f32x4 t = *(const f32x4*)&mls[(lane & 7) * 4];
      float MA = t[0], MB = t[2];
      MA = fmaxf(MA, dpp<XOR1>(MA)); MA = fmaxf(MA, dpp<XOR2>(MA)); MA = fmaxf(MA, dpp<XOR7>(MA));
      MB = fmaxf(MB, dpp<XOR1>(MB)); MB = fmaxf(MB, dpp<XOR2>(MB)); MB = fmaxf(MB, dpp<XOR7>(MB));
      scA = __builtin_amdgcn_exp2f(mA - MA); scB = __builtin_amdgcn_exp2f(mB - MB);
      if (w == 2) {
        float LA = __builtin_amdgcn_exp2f(t[0] - MA) * t[1], LB = __builtin_amdgcn_exp2f(t[2] - MB) * t[3];
        LA += dpp<XOR1>(LA); LA += dpp<XOR2>(LA); LA += dpp<XOR7>(LA);
        LB += dpp<XOR1>(LB); LB += dpp<XOR2>(LB); LB += dpp<XOR7>(LB);
        if (lane < 2) st4(granule(f2u(lane ? MB : MA), f2u(lane ? LB : LA), T0, T1), rws, l16, ms + j * 128);
      }
      FENCE_SCHED();
    }
    if constexpr (i == 47) {
      float u[4];
#pragma unroll
      for (int k = 0; k < 4; ++k) u[k] = swap16_add(aB[k >> 1][k & 1], aB[2 + (k >> 1)][k & 1]);
      *(f32x2*)&part[(8 + w) * 128 + d0] = f32x2{swap32_add(u[0], u[2]) * scB, swap32_add(u[1], u[3]) * scB};
      FENCE_SCHED();
    }
    if constexpr (i == 49) {
      mid_barrier();
      if (w == 1) {
        f32x2 a = *(const f32x2*)&part[8 * 128 + lane * 2];
#pragma unroll
        for (int q = 1; q < 8; ++q) a += *(const f32x2*)&part[(8 + q) * 128 + lane * 2];
        st4(granule(f2u(a[0]), f2u(a[1]), T0, T1), rws, l16, WS_O + (h * 2 + 1) * 8192 + j * 1024);
      }
      FENCE_SCHED();
    }
    if constexpr (i == GB_AT) { gB = ld4<SC1>(rws, gv, gsB); gM = ld4<SC1>(rws, mv, ms); FENCE_SCHED(); }
  });
  TS(6);
  { float u[4];
#pragma unroll
    for (int k = 0; k < 4; ++k) u[k] = swap16_add(aA[k >> 1][k & 1], aA[2 + (k >> 1)][k & 1]);
    *(f32x2*)&part[w * 128 + d0] = f32x2{swap32_add(u[0], u[2]) * scA, swap32_add(u[1], u[3]) * scA}; }
  lds_barrier();
  if (w == 0) {
    f32x2 a = *(const f32x2*)&part[lane * 2];
#pragma unroll
    for (int q = 1; q < 8; ++q) a += *(const f32x2*)&part[q * 128 + lane * 2];
    st4(granule(f2u(a[0]), f2u(a[1]), T0, T1), rws, l16, WS_O + (h * 2) * 8192 + j * 1024);
  }
  FENCE_SCHED();
  TS(7);
  unsigned spins = 0;
  if (__builtin_expect(!all64(valid(gB, T0, T1) & valid(gM, T0, T1)), 0)) {
#pragma nounroll
    do { FENCE_SCHED(); __builtin_amdgcn_s_sleep(4); gB = ld4<SC1>(rws, gv, gsB); gM = ld4<SC1>(rws, mv, ms);
    } while (!all64(valid(gB, T0, T1) & valid(gM, T0, T1)) && ++spins < SPIN_CAP);
  }
  f32x2 y0[4], y1[4], wa[8]; float wA;
  {
    const float m = u2f(gM[0]); float M = fmaxf(m, dpp<XOR1>(m)); M = fmaxf(M, dpp<XOR2>(M));
    const float e = __builtin_amdgcn_exp2f(m - M); float L = e * u2f(gM[2]); L += dpp<XOR1>(L); L += dpp<XOR2>(L);
    const float gt = (lane & 4) ? gate1 : gate0;
    const float wt = e / L * ((lane & 8) ? 1.f - gt : gt), wo_ = dpp<XOR8>(wt);
    wA = (lane & 8) ? wo_ : wt; const float wB = (lane & 8) ? wt : wo_;
#pragma unroll
    for (int k = 0; k < 4; ++k) { y0[k] = f32x2{0.f, 0.f}; y1[k] = f32x2{0.f, 0.f}; }
#pragma unroll
    for (int e2 = 0; e2 < 2; ++e2) {
      float v = u2f(gB[2 * e2]) * wB; v += dpp<XOR1>(v); v += dpp<XOR2>(v);
      const float vo = dpp<XOR7>(v);
#pragma unroll
      for (int k = 0; k < 4; ++k) { const f32x2 wv = up2(S[80 + e2][k]); y0[k] = __builtin_elementwise_fma(f32x2{v, v}, wv, y0[k]); y1[k] = __builtin_elementwise_fma(f32x2{vo, vo}, wv, y1[k]); }
    }
#pragma unroll
    for (int k = 0; k < 8; ++k) { wa[k] = up2(S[82 + (k >> 2)][k & 3]); asm volatile("" : "+v"(wa[k])); }
#pragma unroll
    for (int k = 0; k < 4; ++k) asm volatile("" : "+v"(y0[k]), "+v"(y1[k]));
    asm volatile("" : "+v"(wA));
  }
  FENCE_SCHED();
#pragma nounroll
  do { FENCE_SCHED(); gA = ld4<SC1>(rws, gv, gsA); } while (!all64(valid(gA, T0, T1)) && ++spins < SPIN_CAP);
  TS(8);
  {
#pragma unroll
    for (int e2 = 0; e2 < 2; ++e2) {
      float v = u2f(gA[2 * e2]) * wA; v += dpp<XOR1>(v); v += dpp<XOR2>(v);
      const float vo = dpp<XOR7>(v);
#pragma unroll
      for (int k = 0; k < 4; ++k) { y0[k] = __builtin_elementwise_fma(f32x2{v, v}, wa[e2 * 4 + k], y0[k]); y1[k] = __builtin_elementwise_fma(f32x2{vo, vo}, wa[e2 * 4 + k], y1[k]); }
    }
    float z[8];
#pragma unroll
    for (int k = 0; k < 8; ++k) z[k] = swap32_add(y0[k >> 1][k & 1], y1[k >> 1][k & 1]);
#pragma unroll
    for (int k = 0; k < 4; ++k) z[k] = swap16_add(z[k], z[k + 4]);
    TR(z, 0, 2, XOR8, 8)
    const int o2 = (((lane >> 5) ^ (lane >> 2)) & 1) * 64 + (lane & 7) * 8 + ((lane >> 4) & 1) * 4 + ((lane >> 3) & 1) * 2;
    *(f32x2*)&redy[w * 128 + o2] = f32x2{z[0], z[1]};
  }
  lds_barrier();
  if (w == 0) {
    f32x2 a = *(const f32x2*)&redy[lane * 2];
#pragma unroll
    for (int i = 1; i < 8; ++i) a += *(const f32x2*)&redy[i * 128 + lane * 2];
    st4(granule(f2u(a[0]), f2u(a[1]), T0, T1), rws, l16, WS_Y + j * 32768 + h * 1024);
    FENCE_SCHED();
    TS(9);
    u32x4 gy;
#pragma nounroll
    do { FENCE_SCHED(); gy = ld4<SC1>(rws, (lane >> 1) * 1024 + (lane & 1) * 16, WS_Y + j * 32768 + h * 32); } while (!all64(valid(gy, T0, T1)) && ++spins < SPIN_CAP);
    float y0 = u2f(gy[0]), y1 = u2f(gy[2]);
    y0 += dpp<XOR2>(y0); y1 += dpp<XOR2>(y1);
    y0 += dpp<0x124>(y0); y1 += dpp<0x124>(y1);
    y0 += dpp<XOR8>(y0); y1 += dpp<XOR8>(y1);
    y0 = swap16_add(y0, y0); y1 = swap16_add(y1, y1);
    y0 = swap32_add(y0, y0); y1 = swap32_add(y1, y1);
    const int gl = 2 * h + lane;
    if (lane < 2) ((unsigned*)p.out)[(gl >> 5) * 256 + j * 32 + (gl & 31)] = spins < SPIN_CAP ? pack2(y0, y1) : 0x7fc07fc0u;
    if (g == 0 && lane == 0) __builtin_amdgcn_raw_buffer_store_b64(u32x2{(unsigned)E1, (unsigned)(E1 >> 32)}, rws, 0, WS_EPOCH, SC1);
    TS(10);
  }
}

__global__ void k_pre(unsigned long long* t) { if (threadIdx.x == 0) t[0] = __builtin_amdgcn_s_memrealtime(); }
__global__ void k_post(const unsigned long long* t) { const unsigned long long t0 = t[0]; while (__builtin_amdgcn_s_memrealtime() - t0 < 10000) __builtin_amdgcn_s_sleep(1); }
extern "C" void kernel_launch(void* const* d_in, const int* in_sizes, int n_in, void* d_out, int out_size, void* d_ws, size_t ws_size, hipStream_t stream) {
  P p;
  p.x = d_in[0]; p.kA = d_in[1]; p.vA = d_in[2]; p.wqA = d_in[3]; p.woA = d_in[4];
  p.kB = d_in[6]; p.vB = d_in[7]; p.wqB = d_in[8]; p.woB = d_in[9]; p.gw = d_in[10];
  p.out = d_out; p.ws = d_ws;
  k_pre<<<1,64,0,stream>>>((unsigned long long*)((char*)d_ws + (8u << 20)));
  fused_attn<<<256, 512, 0, stream>>>(p);
  k_post<<<1,64,0,stream>>>((unsigned long long*)((char*)d_ws + (8u << 20)));
}
```

```cpp
#include <hip/hip_runtime.h>
#include <hip/hip_bf16.h>
#include <cstdint>
#include <utility>

typedef unsigned u32x4 __attribute__((ext_vector_type(4)));
typedef unsigned u32x2 __attribute__((ext_vector_type(2)));
typedef unsigned u32x8 __attribute__((ext_vector_type(8)));
typedef unsigned long long u64x8 __attribute__((ext_vector_type(8)));
typedef unsigned long long u64x4 __attribute__((ext_vector_type(4)));
typedef float f32x4 __attribute__((ext_vector_type(4)));
typedef float f32x2 __attribute__((ext_vector_type(2)));
typedef __bf16 bf16x2 __attribute__((ext_vector_type(2)));
typedef __amdgpu_buffer_rsrc_t rsrc_t;
typedef __attribute__((address_space(3))) float lds_f32;
typedef __attribute__((address_space(3))) f32x2 lds_f32x2;
typedef __attribute__((address_space(3))) f32x4 lds_f32x4;

constexpr int WK = 16;
constexpr int WV = 28;
constexpr int POLL_AT = 24;
constexpr int N0 = 40;
constexpr int W0 = 8;
constexpr int CUP = 3;
constexpr int ML_AT = 104;
constexpr int VAL_AT = 124;
constexpr int TAU = 568, TZ = 130, PACE_FROM = 8;
constexpr int WI1 = 148;
constexpr int WD1 = 400;
constexpr int GATE_AT = 7;
constexpr int SD = 3, PS0 = 17, PS = 5;
constexpr int PIN_AT = 132;

constexpr int NT = 2, SC1 = 16;
constexpr unsigned SPIN_CAP = 1u << 20;

typedef unsigned long long u64;
__device__ __forceinline__ rsrc_t mk(u64 p, unsigned n) { return __builtin_amdgcn_make_buffer_rsrc((void*)p, 0, n, 0x00020000); }
template <int AUX> __device__ __forceinline__ u32x4 ld4(rsrc_t r, unsigned v, unsigned s) { return __builtin_amdgcn_raw_buffer_load_b128(r, v, s, AUX); }
template <int AUX> __device__ __forceinline__ u32x2 ld2(rsrc_t r, unsigned v, unsigned s) { return __builtin_amdgcn_raw_buffer_load_b64(r, v, s, AUX); }
__device__ __forceinline__ void st4(u32x4 x, rsrc_t r, unsigned v, unsigned s) { __builtin_amdgcn_raw_buffer_store_b128(x, r, v, s, SC1); }
__device__ __forceinline__ void st4(u32x4 x, u32x4 r, unsigned v, unsigned s) { asm volatile("buffer_store_dwordx4 %0, %1, %2, %3 offen sc1" :: "v"(x), "v"(v), "s"(r), "s"(s) : "memory"); }
__device__ __forceinline__ float dot2(unsigned k, unsigned q, float acc) {
  return __builtin_amdgcn_fdot2_f32_bf16(__builtin_bit_cast(bf16x2, k), __builtin_bit_cast(bf16x2, q), acc, false);
}
template <int CTRL> __device__ __forceinline__ float dpp(float x) {
  return __builtin_bit_cast(float, __builtin_amdgcn_mov_dpp(__builtin_bit_cast(int, x), CTRL, 0xf, 0xf, true));
}
constexpr int XOR1 = 0xB1, XOR2 = 0x4E, XOR7 = 0x141, XOR8 = 0x128, BCAST = 0x150;
__device__ __forceinline__ float blo(unsigned v) { return __builtin_bit_cast(float, v << 16); }
__device__ __forceinline__ float bhi(unsigned v) { return __builtin_bit_cast(float, v & 0xffff0000u); }
__device__ __forceinline__ f32x2 up2(unsigned v) { f32x2 r; r[0] = blo(v); r[1] = bhi(v); return r; }
__device__ __forceinline__ unsigned f2bf(float f) { return __builtin_bit_cast(unsigned short, (__bf16)f); }
__device__ __forceinline__ unsigned pack2(float a, float b) { return f2bf(a) | (f2bf(b) << 16); }
__device__ __forceinline__ unsigned cvt2(float a, float b) { return __builtin_bit_cast(unsigned, __builtin_convertvector(f32x2{a, b}, bf16x2)); }
__device__ __forceinline__ float u2f(unsigned v) { return __builtin_bit_cast(float, v); }
__device__ __forceinline__ unsigned f2u(float v) { return __builtin_bit_cast(unsigned, v); }
__device__ __forceinline__ float swap16_add(float a, float b) {
  auto s = __builtin_amdgcn_permlane16_swap(f2u(a), f2u(b), false, false); return u2f(s[0]) + u2f(s[1]);
}
__device__ __forceinline__ float swap32_add(float a, float b) {
  auto s = __builtin_amdgcn_permlane32_swap(f2u(a), f2u(b), false, false); return u2f(s[0]) + u2f(s[1]);
}
__device__ __forceinline__ float wave_max(float x) {
  x = fmaxf(x, dpp<XOR1>(x)); x = fmaxf(x, dpp<XOR2>(x)); x = fmaxf(x, dpp<XOR7>(x)); x = fmaxf(x, dpp<XOR8>(x));
  auto s = __builtin_amdgcn_permlane16_swap(f2u(x), f2u(x), false, false); x = fmaxf(u2f(s[0]), u2f(s[1]));
  auto t = __builtin_amdgcn_permlane32_swap(f2u(x), f2u(x), false, false); return fmaxf(u2f(t[0]), u2f(t[1]));
}
__device__ __forceinline__ float wave_sum(float x) {
  x += dpp<XOR1>(x); x += dpp<XOR2>(x); x += dpp<XOR7>(x); x += dpp<XOR8>(x);
  x = swap16_add(x, x); return swap32_add(x, x);
}
#define FENCE_SCHED() do { asm volatile("" ::: "memory"); __builtin_amdgcn_sched_barrier(0); } while (0)
template <int N> __device__ __forceinline__ void wait_vm() { asm volatile("s_waitcnt vmcnt(%0)" ::"n"(N) : "memory"); __builtin_amdgcn_sched_barrier(0); }
__device__ __forceinline__ void lds_barrier() { asm volatile("s_waitcnt lgkmcnt(0)\n\ts_barrier" ::: "memory"); __builtin_amdgcn_sched_barrier(0); }

struct P {
  const void *x, *kA, *vA, *wqA, *woA, *kB, *vB, *wqB, *woB, *gw;
  void* out;
  void* ws;
};
constexpr unsigned WS_EPOCH = 0;
constexpr unsigned WS_Q = 4096;
constexpr unsigned WS_ML = WS_Q + 524288;
constexpr unsigned WS_END = WS_ML + 32768;

__device__ __forceinline__ u32x4 granule(unsigned a, unsigned c, unsigned T0, unsigned T1) { u32x4 g; g[0] = a; g[1] = c; g[2] = T0 ^ c; g[3] = T1 ^ a; return g; }
__device__ __forceinline__ bool valid(u32x4 g, unsigned T0, unsigned T1) { return ((g[1] ^ g[2]) == T0) & ((g[3] ^ g[0]) == T1); }
__device__ __forceinline__ unsigned inv(u32x4 g, unsigned T0, unsigned T1) { return (g[1] ^ g[2] ^ T0) | (g[3] ^ g[0] ^ T1); }
__device__ __forceinline__ bool any64(unsigned v) { return __builtin_amdgcn_ballot_w64(v != 0) != 0; }
__device__ __forceinline__ bool all64(bool v) { return __builtin_amdgcn_ballot_w64(v) == ~0ull; }

#define TR(ARR, O, HALF, CTRL, BIT) _Pragma("unroll") for (int q_ = 0; q_ < (HALF); ++q_) { const float lo_ = ARR[(O) + q_], hi_ = ARR[(O) + q_ + (HALF)]; \
    const float t1_ = lo_ + dpp<CTRL>(lo_), t2_ = hi_ + dpp<CTRL>(hi_); ARR[(O) + q_] = (lane & (BIT)) ? t2_ : t1_; }

template <int I> __device__ __forceinline__ void pv(f32x2 (&acc)[4], const u32x4& v, float p) {
  const float pb = dpp<BCAST + I>(p); const f32x2 pp = {pb, pb};
#pragma unroll
  for (int k = 0; k < 4; ++k) acc[k] = __builtin_elementwise_fma(pp, up2(v[k]), acc[k]);
}
template <int I, class F, int... K> __device__ __forceinline__ void unroll_(F&& f, std::integer_sequence<int, K...>) { (f(std::integral_constant<int, I + K>{}), ...); }
template <int I, int N> struct Unroll { template <class F> static __device__ __forceinline__ void run(F&& f) { unroll_<I>(f, std::make_integer_sequence<int, N - I>{}); } };

__device__ __forceinline__ float rbf(float x) { return u2f(f2bf(x) << 16); }
constexpr int issued_after(int i) { int a = N0 + (i + 1) / CUP, b = i + 1 + WV, e = 160 - (159 - WV - i + 1) / 2; if (e < b) b = e; int m = a > b ? a : b; return m < 160 ? m : 160; }
__device__ __forceinline__ unsigned now() { return (unsigned)__builtin_amdgcn_s_memrealtime(); }
__device__ __forceinline__ float rdl(float x, int l) { return u2f(__builtin_amdgcn_readlane(f2u(x), l)); }

__global__ void __launch_bounds__(256, 1) fused_attn(P p_) {
  __shared__ __attribute__((aligned(16))) unsigned qs[128];
  __shared__ float gs;
  __shared__ __attribute__((aligned(16))) float part[4 * 256];
  __shared__ __attribute__((aligned(16))) float redy[4 * 128];
  u64x8 ka; u64x4 kb; unsigned long long t0_;
  asm volatile("s_load_dwordx16 %0, %3, 0x0\n\ts_load_dwordx8 %1, %3, 0x40\n\ts_memrealtime %2" : "=&s"(ka), "=&s"(kb), "=&s"(t0_) : "s"(__builtin_amdgcn_kernarg_segment_ptr()));
  const int g = blockIdx.x, h = (g & 7) * 4 + ((g >> 3) & 3), j = g >> 5, b = j >> 2, s = j & 3;
  const int lane = threadIdx.x & 63, w = __builtin_amdgcn_readfirstlane(threadIdx.x >> 6), r = lane >> 4, c = lane & 15;
  unsigned long long pc; unsigned clen;
  asm volatile("s_getpc_b64 %0\n.Lfa_code0:\n\ts_mov_b32 %1, .Lfa_code1-.Lfa_code0" : "=s"(pc), "=s"(clen));
  const unsigned cpf = __builtin_amdgcn_raw_buffer_load_b32(mk(pc, clen), threadIdx.x * 4, (g >> 3) * 1024, 0);
  FENCE_SCHED();
  const unsigned row0 = s * 128, wqv = (threadIdx.x >> 4) * 65536 + c * 16, xv = (threadIdx.x >> 4) * 16, l16 = lane * 16;
  unsigned wqs[8], k8 = 8192;
  asm volatile("" : "+s"(k8));
  wqs[0] = row0 * 8192 + h * 256;
#pragma unroll
  for (int e = 1; e < 8; ++e) { wqs[e] = wqs[e - 1] + k8; asm volatile("" : "+s"(wqs[e])); }
  const unsigned bh = (unsigned)(b * 32 + h) << 20;
  const unsigned rot = (h >> 2) + b * 8;
  const unsigned oA = bh + s * 16384 + w * 4096, oB = oA + 3072 * 256;
  auto cB = [&](int k) { return oB + ((k + (unsigned)h) & 3) * 65536; };
  auto cA = [&](int k) { return oA + ((k + rot) & 15) * 65536; };
  asm volatile("" :: "s"(wqs[0]), "v"(wqv), "v"(xv), "s"(row0 * 2), "v"(l16));
  asm volatile("" :: "s"(oA), "s"(cB(0)), "s"(cB(1)));
  asm volatile("s_waitcnt lgkmcnt(0)" : "+s"(ka), "+s"(kb), "+s"(t0_));
  const u64 p_x = ka[0], p_kA = ka[1], p_vA = ka[2], p_wqA = ka[3], p_woA = ka[4], p_kB = ka[5], p_vB = ka[6], p_wqB = ka[7], p_woB = kb[0], p_gw = kb[1], p_out = kb[2], p_ws = kb[3];
  const unsigned tz = (unsigned)t0_;
  unsigned tl = tz;
  const rsrc_t rws = mk(p_ws, WS_END), rx = mk(p_x, 2048), rgw = mk(p_gw, 1024);
  const rsrc_t rkA = mk(p_kA, 1u << 26), rvA = mk(p_vA, 1u << 26), rkB = mk(p_kB, 1u << 26), rvB = mk(p_vB, 1u << 26);
  const rsrc_t rwq = mk(b ? p_wqB : p_wqA, 1u << 22), rwo = mk((w & 2) ? p_woB : p_woA, 1u << 22);
  u32x4 wq[8];
  const u32x4 xq0 = ld4<0>(rx, xv, row0 * 2), xq1 = ld4<0>(rx, xv + 1024, row0 * 2);
  FENCE_SCHED();
#pragma unroll
  for (int e = 0; e < 8; ++e) { wq[e] = ld4<NT>(rwq, wqv, wqs[e]); FENCE_SCHED(); }
  const u32x2 ep = ld2<SC1>(rws, 0, WS_EPOCH);
  FENCE_SCHED();
  unsigned wov = (c >> 3) * 16384 + r * 4096 + (c & 7) * 16; const unsigned wos = (h * 128 + (w & 1) * 64) * 1024 + j * 128;
  const unsigned wf = h & 1;
  const u64 ptch = (w & 2 ? (w & 1 ? p_woB : p_woA) : (w & 1 ? p_vA : p_vB)) + (w & 2 ? wos : oB);
  u32x4 S[160], gx, ggw, wo[8]; unsigned tch;
  auto gatef = [&]() {
    float s0 = 0.f;
#pragma unroll
    for (int k = 0; k < 4; ++k) { const f32x2 t0 = up2(gx[k]) * up2(ggw[k]); s0 += rbf(t0[0]) + rbf(t0[1]); }
    s0 = rbf(wave_sum(s0));
    return rbf(1.f / (1.f + __expf(-s0))); };
  auto pace = [&](int due) {
#pragma nounroll
    while ((int)(tl - tz) < due) tl = now();
    FENCE_SCHED(); };
  unsigned wo0 = wos + wf * 32768, wo1 = wos + (wf ^ 1) * 32768;
  auto issue_wo = [&](auto X_, int d) { constexpr int X = X_;
    if (d) pace(TZ + d * TAU / 100);
    asm volatile("" : "+v"(wov));
#pragma unroll
    for (int k = 4 * X; k < 4 * X + 4; ++k) wo[k] = ld4<NT>(rwo, wov + (k & 3) * 1024, X ? wo1 : wo0);
    FENCE_SCHED(); };
  auto issue = [&](auto I) { constexpr int n = I, i = n, k = i % 80;
    if constexpr (n % 4 == 0 && n >= PACE_FROM) pace(TZ + (n / 4) * TAU / 10);
    if constexpr (k < 16) S[i] = ld4<NT>(i < 80 ? rkB : rvB, l16 + (k & 3) * 1024, cB(k >> 2));
    else S[i] = ld4<NT>(i < 80 ? rkA : rvA, l16 + (k & 3) * 1024, cA((k - 16) >> 2));
    if constexpr (n == GATE_AT) { if (w == 3) { gx = ld4<0>(rx, l16, b * 1024); ggw = ld4<0>(rgw, l16, 0); } }
    if constexpr (n == 7) {
      tch = __builtin_amdgcn_raw_buffer_load_b32(mk(ptch, 64), 0, 0, NT) ^ __builtin_amdgcn_raw_buffer_load_b32(rkA, 0, oA, NT);
    }
    if constexpr (n == N0 - 1) asm volatile("" :: "v"(tch));
    if constexpr (n % 4 == 3 && n + 1 >= PACE_FROM && n < 159) tl = now();
    FENCE_SCHED(); };
  Unroll<0, W0>::run(issue);
  {
    f32x2 a0[4], a1[4];
#pragma unroll
    for (int k = 0; k < 4; ++k) { a0[k] = f32x2{0.f, 0.f}; a1[k] = f32x2{0.f, 0.f}; }
    Unroll<0, 8>::run([&](auto E) { constexpr int e = E;
      const float x0 = (e & 1) ? bhi(xq0[e >> 1]) : blo(xq0[e >> 1]), x1 = (e & 1) ? bhi(xq1[e >> 1]) : blo(xq1[e >> 1]);
#pragma unroll
      for (int k = 0; k < 4; ++k) { const f32x2 wv = up2(wq[e][k]); a0[k] = __builtin_elementwise_fma(f32x2{x0, x0}, wv, a0[k]); a1[k] = __builtin_elementwise_fma(f32x2{x1, x1}, wv, a1[k]); }
      asm volatile("" : "+v"(a0[0]), "+v"(a0[1]), "+v"(a0[2]), "+v"(a0[3]), "+v"(a1[0]), "+v"(a1[1]), "+v"(a1[2]), "+v"(a1[3]));
      FENCE_SCHED();
      if constexpr (W0 + e < WK) issue(std::integral_constant<int, W0 + e>{});
    });
    float z[8];
#pragma unroll
    for (int k = 0; k < 8; ++k) z[k] = swap32_add(a0[k >> 1][k & 1], a1[k >> 1][k & 1]);
#pragma unroll
    for (int k = 0; k < 4; ++k) z[k] = swap16_add(z[k], z[k + 4]);
    *(f32x4*)&part[w * 256 + (r >> 1) * 128 + c * 8 + (r & 1) * 4] = f32x4{z[0], z[1], z[2], z[3]};
  }
  const unsigned e_lo = __builtin_amdgcn_readfirstlane(ep[0]), e_hi = __builtin_amdgcn_readfirstlane(ep[1]);
  const unsigned long long E1 = (((unsigned long long)e_hi << 32) | e_lo) + 1, E2 = E1 + 1;
  const unsigned T0 = (unsigned)E1 ^ 0x9E3779B9u, T1 = ((unsigned)(E1 >> 32) ^ 0x7F4A7C15u) + (unsigned)E1 * 0x85EBCA6Bu;
  unsigned U0 = (unsigned)E2 ^ 0x9E3779B9u, U1 = ((unsigned)(E2 >> 32) ^ 0x7F4A7C15u) + (unsigned)E2 * 0x85EBCA6Bu;
  asm volatile("" :: "v"(cpf));
  lds_barrier();
  const unsigned qh = WS_Q + h * 16384;
  if (w < 2) {
    f32x2 a = *(const f32x2*)&part[w * 128 + lane * 2];
#pragma unroll
    for (int i = 1; i < 4; ++i) a += *(const f32x2*)&part[i * 256 + w * 128 + lane * 2];
    st4(granule(f2u(a[0]), f2u(a[1]), T0, T1), rws, l16, qh + j * 2048 + w * 1024);
  }
  if (w == 3) gs = gatef();
  FENCE_SCHED();
  constexpr int N1 = W0 + 8 < WK ? W0 + 8 : (W0 > WK ? W0 : WK);
  Unroll<N1, (WK > N1 ? WK : N1)>::run(issue);
  u32x4 qg[2];
  const unsigned qv = (lane >> 5) * 4096 + (lane & 31) * 16, qoff = qh + (w >> 1) * 8192 + b * 1024 + (w & 1) * 512;
  Unroll<(WK > N1 ? WK : N1), N0>::run([&](auto I) { constexpr int i = I;
    wait_vm<WK - 1>();
    issue(I);
    if constexpr (i == POLL_AT) { qg[0] = ld4<SC1>(rws, qv, qoff); qg[1] = ld4<SC1>(rws, qv + 2048, qoff); FENCE_SCHED(); } });
  unsigned spins = 0;
  {
    if (__builtin_expect(any64(inv(qg[0], T0, T1) | inv(qg[1], T0, T1)), 0)) {
      FENCE_SCHED();
#pragma nounroll
      do { FENCE_SCHED(); qg[0] = ld4<SC1>(rws, qv, qoff); qg[1] = ld4<SC1>(rws, qv + 2048, qoff); ++spins; } while (any64(inv(qg[0], T0, T1) | inv(qg[1], T0, T1)) & (spins < SPIN_CAP));
    }
    float q0 = u2f(qg[0][0]) + u2f(qg[1][0]), q1 = u2f(qg[0][1]) + u2f(qg[1][1]);
    q0 = swap32_add(q0, q0); q1 = swap32_add(q1, q1);
    if (lane < 32) qs[w * 32 + lane] = pack2(q0, q1);
  }
  lds_barrier();
  const u32x4 qA = *(const u32x4*)&qs[c * 4], qB = *(const u32x4*)&qs[64 + c * 4];
  auto refill = [&](auto I) { constexpr int i = I; constexpr int n0 = i ? issued_after(i - 1) : N0, n1 = issued_after(i);
    Unroll<n0, n1>::run([&](auto J) { if constexpr (J - i > WV) wait_vm<WV - 1>(); issue(J); }); };
  float x[80];
  Unroll<0, 80>::run([&](auto I) { constexpr int i = I;
    float d = 0.f;
#pragma unroll
    for (int k = 0; k < 4; ++k) d = dot2(S[i][k], i < 16 ? qB[k] : qA[k], d);
    x[i] = d;
    asm volatile("" : "+v"(x[i]));
    FENCE_SCHED();
    if constexpr (i < 79) refill(I);
    if constexpr (i % 16 == 15) { TR(x, i - 15, 8, XOR8, 8) TR(x, i - 15, 4, XOR7, 4) TR(x, i - 15, 2, XOR2, 2) TR(x, i - 15, 1, XOR1, 1)
      x[i - 15] = rbf(rbf(x[i - 15]) * 0.088388347648318440f) * 1.4426950408889634f; asm volatile("" : "+v"(x[i - 15])); FENCE_SCHED(); }
  });
  const float mB = wave_max(x[0]), mA = wave_max(fmaxf(fmaxf(x[16], x[32]), fmaxf(x[48], x[64])));
  float pr[5];
#pragma unroll
  for (int k = 0; k < 5; ++k) pr[k] = __builtin_amdgcn_exp2f(x[16 * k] - (k ? mA : mB));
  const unsigned mh = WS_ML + h * 1024;
  { const float lA = wave_sum((pr[1] + pr[2]) + (pr[3] + pr[4])), lB = wave_sum(pr[0]);
    if (lane < 2) st4(granule(f2u(lane ? mB : mA), f2u(lane ? lB : lA), T0, T1), rws, l16, mh + j * 128 + w * 32); }
  FENCE_SCHED();
  refill(std::integral_constant<int, 79>{});
  f32x2 aA[4], aB[4], oB2;
#pragma unroll
  for (int k = 0; k < 4; ++k) { aA[k] = f32x2{0.f, 0.f}; aB[k] = f32x2{0.f, 0.f}; }
  float gate, fA;
  u32x4 gM, wt[8];
  const int po = w * 256 + c * 8 + (r & 1) * 4 + (r >> 1) * 2;
  u32x4 wsd = {(unsigned)p_ws, (unsigned)(p_ws >> 32) & 0xffff, WS_END, 0x20000}, outd = {(unsigned)p_out, (unsigned)(p_out >> 32) & 0xffff, 2048, 0x20000};
  unsigned so1 = qh + j * 2048, so2 = so1 + 1024, ps1a = qh + w * 256 + wf * 128, ps1b = qh + w * 256 + (wf ^ 1) * 128, gv = (c >> 1) * 2048 + (c & 1) * 64 + r * 16;
  unsigned pv2 = (lane >> 1) * 16384 + (lane & 1) * 16, ps2 = WS_Q + j * 2048 + 1024 + h * 32;
  unsigned ov = (lane & 30) ? 4096 : (lane & 1) * 4 + (lane >> 5) * 2, os = (h >> 4) * 1024 + j * 128 + (h & 15) * 8;
  unsigned ryw = w * 128 + (r >> 1) * 64 + (c & 7) * 8 + (c >> 3) * 4 + (r & 1) * 2, ryr = lane * 2, w0 = w, w1 = w;
  lds_f32* pryw = (lds_f32*)redy + ryw; const lds_f32 *pryr = (const lds_f32*)redy + ryr, *ppr = (const lds_f32*)part + lane * 4;
  Unroll<80, 160>::run([&](auto I) { constexpr int i = I;
    if constexpr (i == PIN_AT) {
      asm volatile("" : "+s"(wsd), "+s"(outd), "+s"(so1), "+s"(so2), "+s"(ps1a), "+s"(ps1b), "+s"(ps2), "+s"(os), "+s"(U0), "+s"(U1), "+s"(wo0), "+s"(wo1), "+s"(w0), "+s"(w1));
      asm volatile("" : "+v"(gv), "+v"(pv2), "+v"(ov), "+v"(pryw), "+v"(pryr), "+v"(ppr), "+v"(wov));
      FENCE_SCHED(); }
    if constexpr (i < 96) { pv<i % 16>(aB, S[i], pr[0]); asm volatile("" : "+v"(aB[0]), "+v"(aB[1]), "+v"(aB[2]), "+v"(aB[3])); }
    else { pv<i % 16>(aA, S[i], pr[i / 16 - 5]); asm volatile("" : "+v"(aA[0]), "+v"(aA[1]), "+v"(aA[2]), "+v"(aA[3])); }
    FENCE_SCHED();
    refill(I);
    if constexpr (i == ML_AT) { gate = gs; asm volatile("" : "+v"(gate)); FENCE_SCHED(); }
    if constexpr (i == WI1 - 1) { tl = now(); FENCE_SCHED(); }
    if constexpr (i == WI1) issue_wo(std::integral_constant<int, 0>{}, WD1);
    if constexpr (i == 95) {
      float u[4];
#pragma unroll
      for (int k = 0; k < 4; ++k) u[k] = swap16_add(aB[k >> 1][k & 1], aB[2 + (k >> 1)][k & 1]);
      oB2 = f32x2{swap32_add(u[0], u[2]), swap32_add(u[1], u[3])};
      asm volatile("" : "+v"(oB2));
      FENCE_SCHED();
    }
    if constexpr (i == ML_AT) { gM = ld4<SC1>(rws, l16, mh); FENCE_SCHED(); }
    if constexpr (i == VAL_AT) {
      if (__builtin_expect(!all64(valid(gM, T0, T1)), 0)) {
#pragma nounroll
        do { FENCE_SCHED(); gM = ld4<SC1>(rws, l16, mh); } while (!all64(valid(gM, T0, T1)) && ++spins < SPIN_CAP);
        ++spins;
      }
      const float m = u2f(gM[0]);
      float M = fmaxf(m, dpp<XOR2>(m)); M = fmaxf(M, dpp<0x124>(M)); M = fmaxf(M, dpp<XOR8>(M));
      { auto t = __builtin_amdgcn_permlane16_swap(f2u(M), f2u(M), false, false); M = fmaxf(u2f(t[0]), u2f(t[1])); }
      const float e = __builtin_amdgcn_exp2f(m - M);
      float L = e * u2f(gM[1]); L += dpp<XOR2>(L); L += dpp<XOR8>(L); L += dpp<0x124>(L); L = swap16_add(L, L);
      const float f = spins < SPIN_CAP ? e / L * ((lane & 1) ? rbf(1.f - gate) : gate) : u2f(0x7fc00000u);
      fA = rdl(f, j * 8 + w * 2);
      *(f32x2*)&part[po + 128] = oB2 * rdl(f, j * 8 + w * 2 + 1);
      FENCE_SCHED();
    }
  });
  { float u[4];
#pragma unroll
    for (int k = 0; k < 4; ++k) u[k] = swap16_add(aA[k >> 1][k & 1], aA[2 + (k >> 1)][k & 1]);
    *(f32x2*)&part[po] = f32x2{swap32_add(u[0], u[2]), swap32_add(u[1], u[3])} * fA; }
  FENCE_SCHED();
  lds_barrier();
  if (w0 == 0) {
    f32x4 a = *(const lds_f32x4*)ppr;
#pragma unroll
    for (int q = 1; q < 4; ++q) a += *(const lds_f32x4*)(ppr + q * 256);
    st4(granule(cvt2(a[0], a[1]), cvt2(a[2], a[3]), U0, U1), wsd, l16, so1);
  }
  asm volatile("s_cmp_eq_u32 %0, 0\n\ts_cbranch_scc1 .Lfa_sd%=\n\ts_sleep %1\n.Lfa_sd%=:" :: "s"(w0), "n"(SD) : "scc", "memory");
  FENCE_SCHED();
  issue_wo(std::integral_constant<int, 1>{}, 0);
  u32x8 p1;
#define PL1 "buffer_load_dwordx4 v[104:107], %[vo], %[rs], %[s0] offen sc1\n\tbuffer_load_dwordx4 v[100:103], %[vo], %[rs], %[s1] offen sc1\n\t"
#define X3(D, A, B, U) "v_bitop3_b32 %[" #D "], v" #A ", v" #B ", %[" #U "] bitop3:0x96\n\t"
#pragma unroll
  for (int q = 0; q < 4; ++q) {
    if (q == 2) {
      asm volatile("" : "+v"(wo[4]), "+v"(wo[5]), "+v"(wo[6]), "+v"(wo[7])); wait_vm<0>();
      asm volatile(PL1 : "=&{v[100:107]}"(p1) : [vo] "v"(gv), [rs] "s"(wsd), [s0] "s"(ps1a), [s1] "s"(ps1b) : "memory");
      FENCE_SCHED(); }
#pragma unroll
    for (int t = 0; t < 8; ++t) { unsigned v = __builtin_amdgcn_perm(wo[2 * q + 1][t >> 1], wo[2 * q][t >> 1], (t & 1) ? 0x07060302u : 0x05040100u);
      asm volatile("" : "+v"(v)); wt[t][q] = v; }
    FENCE_SCHED();
  }
  asm volatile("" : "+v"(wt[0]), "+v"(wt[1]), "+v"(wt[2]), "+v"(wt[3]), "+v"(wt[4]), "+v"(wt[5]), "+v"(wt[6]), "+v"(wt[7]));
  {
    float u0, u1, u2, u3;
#define MM(t, n) "v_mfma_f32_16x16x32_bf16 v[" #t ":" #t "+3], v[98:101], %[w" #n "], 0\n\t"
#define PK(t) "v_pk_add_f32 v[" #t ":" #t "+1], v[" #t ":" #t "+1], v[" #t "+2:" #t "+3]\n\t"
#define LO(d, t) "v_add_f32_dpp %[t" #d "], v[" #t "+1], v[" #t "] row_ror:8 row_mask:0xf bank_mask:0x3\n\t"
#define HI(d, t) "v_add_f32_dpp %[t" #d "], v[" #t "], v[" #t "+1] row_ror:8 row_mask:0xf bank_mask:0xc\n\t"
    asm volatile("s_branch .Lfa_h1w\n.Lfa_h1r:\n\t" PL1
      "s_add_u32 %[sp], %[sp], 1\n\ts_cmp_lt_u32 %[sp], %[cap]\n\ts_cbranch_scc1 .Lfa_h1w\n\t"
      "s_waitcnt vmcnt(0)\n\tv_mov_b32 v98, 0x7fc07fc0\n\ts_nop 1\n\ts_branch .Lfa_h1d\n"
      ".Lfa_h1w:\n\ts_waitcnt vmcnt(0)\n\tv_pk_mov_b32 v[98:99], v[104:105], v[104:105] op_sel:[0,1]\n\t"
      X3(t0, 105, 106, u0) X3(t1, 107, 104, u1) MM(128, 0) X3(t2, 101, 102, u0) X3(t3, 103, 100, u1) MM(144, 4)
      "v_or3_b32 %[t0], %[t0], %[t1], %[t2]\n\tv_or_b32 %[t0], %[t0], %[t3]\n\t" MM(136, 2) "v_cmp_ne_u32 vcc, 0, %[t0]\n\ts_cbranch_vccnz .Lfa_h1r\n.Lfa_h1d:\n\t"
      MM(152, 6) PK(128) "s_nop 0\n\t" MM(132, 1) PK(144) LO(0, 128) MM(148, 5) PK(136) HI(0, 144) MM(140, 3) PK(152) LO(2, 136) MM(156, 7)
      PK(132) HI(2, 152) PK(148) LO(1, 132) "v_permlane16_swap_b32 %[t0], %[t2]\n\t" PK(140) HI(1, 148) "v_add_f32 v96, %[t0], %[t2]\n\t" PK(156) LO(3, 140)
      "s_nop 1\n\t" HI(3, 156) "s_nop 1\n\tv_permlane16_swap_b32 %[t1], %[t3]\n\tv_add_f32 v97, %[t1], %[t3]\n\tds_write_b64 %[ry], v[96:97]"
      : [t0] "=&v"(u0), [t1] "=&v"(u1), [t2] "=&v"(u2), [t3] "=&v"(u3), [sp] "+s"(spins), "+{v[100:107]}"(p1)
      : [w0] "v"(wt[0]), [w1] "v"(wt[1]), [w2] "v"(wt[2]), [w3] "v"(wt[3]), [w4] "v"(wt[4]), [w5] "v"(wt[5]), [w6] "v"(wt[6]), [w7] "v"(wt[7]),
        [vo] "v"(gv), [rs] "s"(wsd), [s0] "s"(ps1a), [s1] "s"(ps1b), [u0] "s"(U0), [u1] "s"(U1), [cap] "n"(SPIN_CAP), [ry] "v"(pryw)
      : "vcc", "scc", "memory", "v96", "v97", "v98", "v99",
        "v128", "v129", "v130", "v131", "v132", "v133", "v134", "v135", "v136", "v137", "v138", "v139", "v140", "v141", "v142", "v143",
        "v144", "v145", "v146", "v147", "v148", "v149", "v150", "v151", "v152", "v153", "v154", "v155", "v156", "v157", "v158", "v159");
  }
  {
  lds_barrier();
  if (__builtin_expect(w1 == 0, 1)) {
    const lds_f32x2* pq = (const lds_f32x2*)pryr;
    const f32x2 a = (pq[0] + pq[64]) + (pq[128] + pq[192]);
    st4(granule(f2u(a[0]), f2u(a[1]), U0, U1), wsd, l16, so2);
    FENCE_SCHED();
  }
  if (w0 == 1) {
    float y, z; unsigned t0, t1; u32x8 p2;
#define AD(d, a, ctl) "v_add_f32_dpp " d ", " a ", " a " " ctl " row_mask:0xf bank_mask:0xf\n\t"
#define PL2(R) "buffer_load_dwordx4 v[" #R ":" #R "+3], %[vo], %[rs], %[so] offen sc1\n\t"
#define CK2(R) "s_waitcnt vmcnt(1)\n\t" AD("%[y]", "v[" #R "]", "row_ror:8") AD("%[z]", "v[" #R "+1]", "row_ror:8") X3(t0, [R+1], [R+2], u0) X3(t1, [R+3], [R], u1) \
      "v_permlane32_swap_b32 %[y], %[z]\n\tv_add_f32 %[y], %[y], %[z]\n\tv_or_b32 %[t0], %[t0], %[t1]\n\tv_cmp_ne_u32 vcc, 0, %[t0]\n\t" AD("%[y]", "%[y]", "row_ror:4") \
      "s_cbranch_vccz .Lfa_h2d\n\t" PL2(R)
    asm volatile("s_sleep %[s0]\n\t" PL2(128)
      "s_sleep %[s1]\n\t" PL2(132)
      "\n.Lfa_h2r:\n\t" CK2(128)
      CK2(132)
      "s_add_u32 %[sp], %[sp], 1\n\ts_cmp_lt_u32 %[sp], %[cap]\n\ts_cbranch_scc1 .Lfa_h2r\n\tv_mov_b32 %[y], 0x7fc00000\n\ts_nop 0\n.Lfa_h2d:\n\ts_nop 0\n\t"
      AD("%[z]", "%[y]", "quad_perm:[2,3,0,1]") AD("%[y]", "%[y]", "quad_perm:[2,3,0,1]")
      "s_nop 1\n\tv_permlane16_swap_b32 %[y], %[z]\n\tv_add_f32 %[y], %[y], %[z]\n\tv_cvt_pk_bf16_f32 %[y], %[y], %[y]\n\t"
      "buffer_store_short %[y], %[ov], %[od], %[os] offen"
      : [y] "=&v"(y), [z] "=&v"(z), [t0] "=&v"(t0), [t1] "=&v"(t1), [sp] "+s"(spins), "=&{v[128:135]}"(p2)
      : [vo] "v"(pv2), [rs] "s"(wsd), [so] "s"(ps2), [u0] "s"(U0), [u1] "s"(U1), [cap] "n"(SPIN_CAP), [ov] "v"(ov), [od] "s"(outd), [os] "s"(os), [s0] "n"(PS0), [s1] "n"(PS)
      : "vcc", "scc", "memory");
    if (g == 0 && lane == 0) __builtin_amdgcn_raw_buffer_store_b64(u32x2{(unsigned)E2, (unsigned)(E2 >> 32)}, rws, 0, WS_EPOCH, SC1);
    asm volatile("s_waitcnt vmcnt(0)" :: "{v[128:135]}"(p2) : "memory");
  }
  }
  asm volatile(".Lfa_code1:");
}

extern "C" void kernel_launch(void* const* d_in, const int* in_sizes, int n_in, void* d_out, int out_size, void* d_ws, size_t ws_size, hipStream_t stream) {
  P p;
  p.x = d_in[0]; p.kA = d_in[1]; p.vA = d_in[2]; p.wqA = d_in[3]; p.woA = d_in[4];
  p.kB = d_in[6]; p.vB = d_in[7]; p.wqB = d_in[8]; p.woB = d_in[9]; p.gw = d_in[10];
  p.out = d_out; p.ws = d_ws;
  fused_attn<<<256, 256, 0, stream>>>(p);
}
```
